```python
import torch

def _softmax(x, axis=-1):
    return torch.softmax(x, dim=axis)
def _gelu(x):
    return torch.nn.functional.gelu(x, approximate="tanh")
def _silu(x):
    return torch.nn.functional.silu(x)
def _sigmoid(x):
    return torch.sigmoid(x)
def _layernorm(x, axis=-1, eps=1e-5):
    m = x.mean(dim=axis, keepdim=True)
    v = x.var(dim=axis, keepdim=True, unbiased=False)
    return (x - m) / torch.sqrt(v + eps)
def _scatter_add(tgt, idx, src, axis=0):
    return tgt.clone().index_add_(axis, idx, src)
def _segment_sum(src, seg, n):
    out = torch.zeros((n,) + src.shape[1:], dtype=src.dtype)
    return out.index_add_(0, seg, src)
def _pad(x, pw):
    flat = tuple(v for pair in reversed(pw) for v in pair)
    return torch.nn.functional.pad(x, flat)

kernel_name = 'interleave_layers__sliding_block__glu_gated_swiglu__prompt'

def setup_inputs():

    _w = (lambda a: (lambda t: t.to(torch.bfloat16) if t.is_floating_point() else t)(torch.as_tensor(a)))
    def _A_setup():
        B, S, D = 1, 2048, 640
        return {
            "x": _w((lambda *s: torch.randn(*s, dtype=torch.bfloat16) * 0.05)(B, S, D)),
            "wqkv": _w((lambda *s: torch.randn(*s, dtype=torch.bfloat16) * 0.05)(D, 3 * D)),
            "wo": _w((lambda *s: torch.randn(*s, dtype=torch.bfloat16) * 0.05)(D, D)),
        }
    _A_in = _A_setup()

    def _B_setup():
        B, S, D, F = 1, 2048, 640, 1728
        return {
            "x": _w((lambda *s: torch.randn(*s, dtype=torch.bfloat16) * 0.05)(B, S, D)),
            "w1": _w((lambda *s: torch.randn(*s, dtype=torch.bfloat16) * 0.8)(D, F) * (2.0 / D) ** 0.5),
            "w3": _w((lambda *s: torch.randn(*s, dtype=torch.bfloat16) * 0.8)(D, F) * (2.0 / D) ** 0.5),
            "w2": _w((lambda *s: torch.randn(*s, dtype=torch.bfloat16) * 0.8)(F, D) * (2.0 / F) ** 0.5),
        }
    _B_in = _B_setup()

    return {
        "A_x": _A_in["x"],
        "A_wqkv": _A_in["wqkv"],
        "A_wo": _A_in["wo"],
        "B_x": _B_in["x"],
        "B_w1": _B_in["w1"],
        "B_w3": _B_in["w3"],
        "B_w2": _B_in["w2"],
    }

def reference(A_x, A_wqkv, A_wo, B_x, B_w1, B_w3, B_w2):
    def _A_ref(x, wqkv, wo):
        B, S, D = x.shape
        H = 32; Dh = D // H; W = 256
        qkv = (lambda x, *d: x.permute(*d))((x @ wqkv).reshape(B, S, 3, H, Dh), 2, 0, 3, 1, 4)
        q, k, v = qkv[0], qkv[1], qkv[2]
        scores = torch.einsum("bhqd,bhkd->bhqk", q, k) * (Dh ** -0.5)
        idx = torch.arange(S)
        mask = (idx[:, None] - idx[None, :] >= 0) & (idx[:, None] - idx[None, :] < W)
        scores = torch.where(mask[None, None], scores, -1e9)
        p = _softmax(scores, axis=-1)
        o = torch.einsum("bhqk,bhkd->bhqd", p, v)
        return (lambda x, *d: x.permute(*d))(o, 0, 2, 1, 3).reshape(B, S, D) @ wo

    def _B_ref(x, w1, w3, w2):
        gate = torch.einsum("bsd,df->bsf", x, w1)
        up = torch.einsum("bsd,df->bsf", x, w3)
        h = _silu(gate) * up
        out = torch.einsum("bsf,fd->bsd", h, w2)
        return out

    x = A_x
    x = x + _A_ref(_layernorm(x, axis=-1), A_wqkv, A_wo)
    x = x + _B_ref(_layernorm(x, axis=-1), B_w1, B_w3, B_w2)
    x = x + _A_ref(_layernorm(x, axis=-1), A_wqkv, A_wo)
    x = x + _B_ref(_layernorm(x, axis=-1), B_w1, B_w3, B_w2)
    return x
```

```cpp
#include <hip/hip_runtime.h>
#include <stdint.h>

#define LAS __attribute__((address_space(3)))
#define GAS __attribute__((address_space(1)))
#define DI __device__ __forceinline__
typedef unsigned short u16;
typedef short bf16x8 __attribute__((ext_vector_type(8)));
typedef short s16x4 __attribute__((ext_vector_type(4)));
typedef float f32x2 __attribute__((ext_vector_type(2)));
typedef float f32x4 __attribute__((ext_vector_type(4)));
typedef float f32x16 __attribute__((ext_vector_type(16)));
typedef unsigned u32x2 __attribute__((ext_vector_type(2)));
typedef unsigned u32x4 __attribute__((ext_vector_type(4)));
typedef __bf16 bf16x2_t __attribute__((ext_vector_type(2)));
typedef GAS unsigned gu32;
#define RLX_AGENT __ATOMIC_RELAXED, __HIP_MEMORY_SCOPE_AGENT
constexpr int AUX_SC1 = 16;

constexpr int SEQ = 2048, DM = 640, FF = 1728;
constexpr float LN_EPS = 1e-5f;

constexpr unsigned KB = 1024;
constexpr unsigned WS_FLAGS = 0;
constexpr unsigned WS_TMO = 24 * KB;
constexpr unsigned WS_CSQ = 32 * KB;
constexpr unsigned WS_CS13 = 40 * KB;
constexpr unsigned WS_WQ = 64 * KB;
constexpr unsigned WS_WO = WS_WQ + 16 * 160 * KB;
constexpr unsigned WS_W13 = WS_WO + 8 * 100 * KB;
constexpr unsigned WS_W2 = WS_W13 + 16 * 280 * KB;
constexpr unsigned XB = 128 * 20 * KB;
constexpr unsigned WS_X = WS_W2 + 8 * 270 * KB;
constexpr unsigned STB = 128 * KB;
constexpr unsigned WS_ST = WS_X + 4 * XB;
constexpr unsigned KVB = 22 * KB;
constexpr unsigned WS_KV = WS_ST + 4 * STB;
constexpr unsigned WS_O = WS_KV + 2 * 256 * KVB;
constexpr unsigned HB = 128 * 54 * KB;
constexpr unsigned WS_H = WS_O + 2 * XB;
constexpr unsigned WS_END = WS_H + 2 * HB;

constexpr unsigned RING_KB = 80;
constexpr unsigned L_ATT = RING_KB * KB;
constexpr unsigned L_KA = L_ATT, L_KBB = L_KA + 12288, L_KT = L_KBB + 12288;
constexpr unsigned L_VA = L_KT + 6144, L_VB = L_VA + 12288, L_VT = L_VB + 12288 + 128;
constexpr unsigned L_QM = L_VT + 12288, L_QT = L_QM + 8192;
constexpr unsigned L_CSQ = L_QT + 2048, L_CS13 = L_CSQ + 512, L_END = L_CS13 + 896;
constexpr unsigned L_XCH = L_ATT;
constexpr unsigned LDS_BYTES = 160 * KB;
static_assert(L_END <= LDS_BYTES, "LDS map");
static_assert((L_VT - L_VA) % 256 == 128 && (L_VT - L_VB) % 256 == 128, "V banks");

constexpr int NCH = 49;
constexpr int C1 = 0, C3 = 10, C4 = 15, C5 = 35;
constexpr int ch_size(int n) { return n < C3 ? 16 : n < C4 ? 20 : n < C5 ? 14 : n < NCH - 1 ? 20 : 10; }
constexpr int ch_phase(int n) { return n < C3 ? 0 : n < C4 ? 1 : n < C5 ? 2 : 3; }
constexpr int ch_src(int n) { return n < C3 ? n * 16 : n < C4 ? (n - C3) * 20 : n < C5 ? (n - C4) * 14 : (n - C5) * 20; }
constexpr int NEV = 10 + 4 + 5 + 2 + 20 + 1 + 14 + 2;
constexpr int EV_B1 = 0, EV_E1 = 10, EV_B3 = 14, EV_X3 = 19, EV_B4 = 21, EV_E6 = 41, EV_B5 = 42, EV_X5 = 56;
struct Sched { int pos[NCH]; int need[NEV]; int cons[NEV]; int wait[NEV]; int fb[NEV]; int fe[NEV]; int f0; bool ok; };
constexpr int ch_cnt(int n, int lw) { int p = (n & 1) * 2, c = 0; for (int i = 0; i < ch_size(n); ++i) if ((i & 3) == ((lw + p) & 3)) ++c; return c; }
constexpr Sched make_sched() {
    Sched s{};
    int p = 0;
    for (int n = 0; n < NCH; ++n) { if (p + ch_size(n) > (int)RING_KB) p = 0; s.pos[n] = p; p += ch_size(n); }
    for (int e = 0; e < NEV; ++e) { s.need[e] = -1; s.cons[e] = 0; }
    for (int c = 0; c < 10; ++c) { s.need[EV_B1 + c] = C1 + c; s.cons[EV_B1 + c] = C1 + c; }
    for (int e = EV_E1; e < EV_B3; ++e) s.cons[e] = C3;
    for (int c = 0; c < 5; ++c) { s.need[EV_B3 + c] = C3 + c; s.cons[EV_B3 + c] = C3 + c; }
    for (int e = EV_X3; e < EV_B4; ++e) s.cons[e] = C4;
    for (int c = 0; c < 20; ++c) { s.need[EV_B4 + c] = C4 + c; s.cons[EV_B4 + c] = C4 + c; }
    s.cons[EV_E6] = C5;
    for (int c = 0; c < 14; ++c) { s.need[EV_B5 + c] = C5 + c; s.cons[EV_B5 + c] = C5 + c; }
    for (int e = EV_X5; e < NEV; ++e) s.cons[e] = NCH;
    int f0 = 0; { int q = 0; while (f0 < NCH && s.pos[f0] == q) { q += ch_size(f0); ++f0; } }
    s.f0 = f0; s.ok = true;
    int f = f0;
    for (int e = 0; e < NEV; ++e) {
        s.wait[e] = 63;
        if (s.need[e] >= 0) {
            if (s.need[e] >= f) s.ok = false;
            int k = 63;
            for (int lw = 0; lw < 4; ++lw) { int c = 0; for (int m = s.need[e] + 1; m < f; ++m) c += ch_cnt(m % NCH, lw); if (c < k) k = c; }
            s.wait[e] = k;
        }
        s.fb[e] = f;
        while (f < NCH + f0) {
            const int m = f % NCH, lo = s.pos[m], hi = lo + ch_size(m);
            bool free_ = true;
            for (int n = s.cons[e]; n < f; ++n) { const int q = n % NCH, a = s.pos[q], b = a + ch_size(q); if (a < hi && lo < b) free_ = false; }
            if (!free_) break;
            ++f;
        }
        s.fe[e] = f;
    }
    if (f != NCH + f0) s.ok = false;
    return s;
}
constexpr Sched SC = make_sched();
static_assert(SC.ok, "ring schedule");

DI unsigned pk_bf16(float lo, float hi) { f32x2 v = {lo, hi}; bf16x2_t b = __builtin_convertvector(v, bf16x2_t); return __builtin_bit_cast(unsigned, b); }
DI float bf_lo(unsigned u) { return __uint_as_float(u << 16); }
DI float bf_hi(unsigned u) { return __uint_as_float(u & 0xffff0000u); }
DI float bf2f(u16 v) { return __uint_as_float((unsigned)v << 16); }
DI bf16x8 as_frag(u32x4 v) { return __builtin_bit_cast(bf16x8, v); }
#define MFMA16(a, b, c) __builtin_amdgcn_mfma_f32_16x16x32_bf16((a), (b), (c), 0, 0, 0)
#define MFMA32(a, b, c) __builtin_amdgcn_mfma_f32_32x32x16_bf16((a), (b), (c), 0, 0, 0)
#define BAR_LGKM() asm volatile("s_waitcnt lgkmcnt(0)\n\ts_barrier" ::: "memory")
#define BAR_ALL() asm volatile("s_waitcnt vmcnt(0) lgkmcnt(0)\n\ts_barrier" ::: "memory")
#define SBAR() __builtin_amdgcn_sched_barrier(0)
typedef const LAS char* lds_cptr;
typedef LAS char* lds_ptr;
DI bf16x8 lds_frag(lds_cptr p) { return *(const LAS bf16x8*)p; }
DI float swap32_max(float m) { auto r = __builtin_amdgcn_permlane32_swap(__float_as_uint(m), __float_as_uint(m), false, false); return fmaxf(__uint_as_float(r[0]), __uint_as_float(r[1])); }
DI float swap32_sum(float m) { auto r = __builtin_amdgcn_permlane32_swap(__float_as_uint(m), __float_as_uint(m), false, false); return __uint_as_float(r[0]) + __uint_as_float(r[1]); }
DI float swap16_sum(float m) { auto r = __builtin_amdgcn_permlane16_swap(__float_as_uint(m), __float_as_uint(m), false, false); return __uint_as_float(r[0]) + __uint_as_float(r[1]); }
DI float sum_q(float v) { return swap32_sum(swap16_sum(v)); }

struct PrepArgs { const u16* x; const u16* wqkv; const u16* wo; const u16* w1; const u16* w3; const u16* w2; char* ws; };

DI int outfeat(int c8, int t, int r) {
    const int q = r >> 2, i = r & 3;
    if (t == 4) { const int gt = (c8 & 1) ? 5 * c8 : 5 * c8 + 4; return 16 * gt + r; }
    const int s = ((5 * c8 + 1) >> 1) + (t >> 1);
    return 32 * s + 8 * q + 4 * (t & 1) + i;
}
DI int qkvcol(int j, int t, int r) {
    const int q = r >> 2, i = r & 3;
    if (t < 6) return (t >> 1) * DM + (2 * j + (q >> 1)) * 20 + 8 * (q & 1) + 4 * (t & 1) + i;
    if (t == 6) return (q >> 1) * DM + (2 * j + (q & 1)) * 20 + 16 + i;
    return q < 2 ? 2 * DM + (2 * j + q) * 20 + 16 + i : -1;
}
DI int orow(int s, int q, int e) {
    const int c = 4 * s + q, j = c / 5, i = c % 5;
    if (i < 4) { const int h = i & 1; return (2 * j + (i >> 1)) * 20 + (e < 4 ? 4 * h + e : 8 + 4 * h + e - 4); }
    return (2 * j + (e >> 2)) * 20 + 16 + (e & 3);
}
DI int hrow(int s, int q, int e) { const int f = 2 * s + (q >> 1), h = q & 1; return 16 * f + (e < 4 ? 4 * h + e : 8 + 4 * h + e - 4); }
DI int f_first(int j) { return 27 * (j >> 2) + 7 * (j & 3); }

__global__ __launch_bounds__(256) void prep_kernel(PrepArgs a) {
    __shared__ float red[4][64];
    const int u = blockIdx.x, wave = __builtin_amdgcn_readfirstlane(threadIdx.x >> 6), lane = threadIdx.x & 63;
    if (u < 160 && threadIdx.x < 32) ((unsigned*)(a.ws + WS_FLAGS))[u * 32 + threadIdx.x] = 0u;
    if (u == 160 && threadIdx.x < 32) ((unsigned*)(a.ws + WS_TMO))[threadIdx.x] = 0u;
    if (u < 128) {
        const int j = u >> 3, t = u & 7, r = lane & 15, q = lane >> 4, col = qkvcol(j, t, r);
        float cs = 0.f;
        for (int s = wave * 5; s < wave * 5 + 5; ++s) {
            u16 v[8];
#pragma unroll
            for (int e = 0; e < 8; ++e) v[e] = col >= 0 ? a.wqkv[(size_t)(32 * s + 8 * q + e) * (3 * DM) + col] : (u16)0;
            u32x4 o;
#pragma unroll
            for (int e = 0; e < 4; ++e) { o[e] = v[2 * e] | ((unsigned)v[2 * e + 1] << 16); cs += bf2f(v[2 * e]) + bf2f(v[2 * e + 1]); }
            *(u32x4*)(a.ws + WS_WQ + j * 160 * KB + (s * 8 + t) * KB + lane * 16) = o;
        }
        cs = sum_q(cs);
        red[wave][lane] = cs; __syncthreads();
        if (threadIdx.x < 16) ((float*)(a.ws + WS_CSQ))[j * 128 + t * 16 + threadIdx.x] = (red[0][threadIdx.x] + red[1][threadIdx.x]) + (red[2][threadIdx.x] + red[3][threadIdx.x]);
    } else if (u < 240) {
        const int v_ = u - 128, j = v_ / 7, t = v_ % 7, r = lane & 31, h = lane >> 5;
        int f = f_first(j) + t; if (f > 107) f = 107; if ((j & 3) == 3 && t == 6) f = f_first(j) + 5;
        const u16* W = r < 16 ? a.w1 : a.w3; const int col = 16 * f + (r & 15);
        float cs = 0.f;
        for (int s = wave * 10; s < wave * 10 + 10; ++s) {
            u16 v[8];
#pragma unroll
            for (int e = 0; e < 8; ++e) v[e] = W[(size_t)(16 * s + 8 * h + e) * FF + col];
            u32x4 o;
#pragma unroll
            for (int e = 0; e < 4; ++e) { o[e] = v[2 * e] | ((unsigned)v[2 * e + 1] << 16); cs += bf2f(v[2 * e]) + bf2f(v[2 * e + 1]); }
            *(u32x4*)(a.ws + WS_W13 + j * 280 * KB + (s * 7 + t) * KB + lane * 16) = o;
        }
        cs = swap32_sum(cs);
        red[wave][lane] = cs; __syncthreads();
        if (threadIdx.x < 32) ((float*)(a.ws + WS_CS13))[j * 224 + t * 32 + threadIdx.x] = (red[0][threadIdx.x] + red[1][threadIdx.x]) + (red[2][threadIdx.x] + red[3][threadIdx.x]);
    } else if (u < 280) {
        const int v_ = u - 240, c8 = v_ / 5, t = v_ % 5, r = lane & 15, q = lane >> 4, col = outfeat(c8, t, r);
        for (int s = wave * 5; s < wave * 5 + 5; ++s) {
            u16 v[8];
#pragma unroll
            for (int e = 0; e < 8; ++e) v[e] = a.wo[(size_t)orow(s, q, e) * DM + col];
            u32x4 o;
#pragma unroll
            for (int e = 0; e < 4; ++e) o[e] = v[2 * e] | ((unsigned)v[2 * e + 1] << 16);
            const int kh = s / 10, sl = s % 10;
            *(u32x4*)(a.ws + WS_WO + c8 * 100 * KB + ((sl >> 1) * 20 + (kh * 2 + (sl & 1)) * 5 + t) * KB + lane * 16) = o;
        }
    } else if (u < 320) {
        const int v_ = u - 280, c8 = v_ / 5, t = v_ % 5, r = lane & 15, q = lane >> 4, col = outfeat(c8, t, r);
        for (int s = wave; s < 54; s += 4) {
            u16 v[8];
#pragma unroll
            for (int e = 0; e < 8; ++e) v[e] = a.w2[(size_t)hrow(s, q, e) * DM + col];
            u32x4 o;
#pragma unroll
            for (int e = 0; e < 4; ++e) o[e] = v[2 * e] | ((unsigned)v[2 * e + 1] << 16);
            const int kh = s / 27, sl = s % 27;
            const int off = sl < 26 ? (sl >> 1) * 20 + (kh * 2 + (sl & 1)) * 5 + t : 13 * 20 + kh * 5 + t;
            *(u32x4*)(a.ws + WS_W2 + c8 * 270 * KB + off * KB + lane * 16) = o;
        }
    } else {
        const int tt = u - 320, tok = lane & 15, q = lane >> 4;
        u32x4 v[5]; float sum = 0.f;
#pragma unroll
        for (int i = 0; i < 5; ++i) {
            const int s = wave * 5 + i;
            v[i] = *(const u32x4*)(a.x + (size_t)(16 * tt + tok) * DM + 32 * s + 8 * q);
            *(u32x4*)(a.ws + WS_X + (tt * 20 + s) * KB + lane * 16) = v[i];
#pragma unroll
            for (int e = 0; e < 4; ++e) sum += bf_lo(v[i][e]) + bf_hi(v[i][e]);
        }
        sum = sum_q(sum);
        red[wave][lane] = sum; __syncthreads();
        const float mean = ((red[0][tok] + red[1][tok]) + (red[2][tok] + red[3][tok])) * (1.f / DM);
        __syncthreads();
        float m2 = 0.f;
#pragma unroll
        for (int i = 0; i < 5; ++i)
#pragma unroll
            for (int e = 0; e < 4; ++e) { const float d0 = bf_lo(v[i][e]) - mean, d1 = bf_hi(v[i][e]) - mean; m2 += d0 * d0 + d1 * d1; }
        m2 = sum_q(m2);
        red[wave][lane] = m2; __syncthreads();
        if (threadIdx.x < 128) {
            const int c8 = threadIdx.x >> 4, tk = threadIdx.x & 15;
            const float mean_t = __shfl(mean, tk);
            const float tot = (red[0][tk] + red[1][tk]) + (red[2][tk] + red[3][tk]);
            const int g = tt >> 3, tl = (tt & 7) * 16 + tk;
            *(f32x2*)(a.ws + WS_ST + ((g * 8 + c8) * 128 + tl) * 8) = (f32x2){mean_t, c8 == 0 ? tot : 0.f};
        }
    }
}

struct MainArgs { char* ws; u16* out; };

struct Ctx {
    __amdgpu_buffer_rsrc_t rs;
    char* ws; u16* out;
    lds_ptr lds; unsigned lds0;
    int g, j, wave, lane;
};

DI Ctx fresh(const Ctx& c0) {
    Ctx c = c0;
    asm volatile("" : "+v"(c.lane), "+s"(c.g), "+s"(c.j), "+s"(c.wave), "+s"(c.lds0));
    return c;
}
DI u32x4 ld128(const Ctx& c, unsigned voff, unsigned soff) { return __builtin_amdgcn_raw_buffer_load_b128(c.rs, voff, soff, AUX_SC1); }
DI u32x2 ld64(const Ctx& c, unsigned voff, unsigned soff) { return __builtin_amdgcn_raw_buffer_load_b64(c.rs, voff, soff, AUX_SC1); }
DI void st128(const Ctx& c, u32x4 v, unsigned voff, unsigned soff) { __builtin_amdgcn_raw_buffer_store_b128(v, c.rs, voff, soff, AUX_SC1); }
DI void st64(const Ctx& c, u32x2 v, unsigned voff, unsigned soff) { __builtin_amdgcn_raw_buffer_store_b64(v, c.rs, voff, soff, AUX_SC1); }

DI unsigned flag_off(int L, int ph, int g) { return WS_FLAGS + ((L * 5 + ph) * 16 + g) * 128; }
DI void publish(const Ctx& c, int L, int ph) {
    if (c.wave == 0 && c.lane == 0) __hip_atomic_store((gu32*)(c.ws + flag_off(L, ph, c.g)) + c.j, 1u, RLX_AGENT);
}
DI void poll(const Ctx& c, unsigned off, bool use) {
    if (c.wave != 0) return;
    for (unsigned spins = 0;; ++spins) {
        const unsigned v = use ? __hip_atomic_load((gu32*)(c.ws + off), RLX_AGENT) : 1u;
        if (__all(v == 1u)) break;
        if (spins > (1u << 20)) { if (c.lane == 0) __hip_atomic_store((gu32*)(c.ws + WS_TMO), 1u, RLX_AGENT); break; }
        __builtin_amdgcn_s_sleep(1);
    }
}
DI void poll_group(const Ctx& c, int L, int ph) { poll(c, flag_off(L, ph, c.g) + (c.lane & 15) * 4, true); }

struct Loader { unsigned voff; unsigned src[4]; int lw; };
template <int M> DI void fill_chunk(const Ctx& c, const Loader& l) {
    constexpr int n = M % NCH, size = ch_size(n), p = (n & 1) * 2;
    const unsigned rot = (unsigned)((l.lw + p) & 3);
#pragma unroll
    for (int i0 = 0; i0 < (size + 3) / 4; ++i0) {
        const unsigned i = 4 * i0 + rot;
        if (4 * i0 + 3 < size || i < (unsigned)size) {
            const unsigned dst = c.lds0 + (SC.pos[n] + 4 * i0) * KB + rot * KB, src = l.src[ch_phase(n)] + (ch_src(n) + 4 * i0) * KB + rot * KB;
            asm volatile("s_mov_b32 m0, %0\n\ts_nop 0\n\tbuffer_load_dwordx4 %1, %2, %3 offen lds" :: "s"(dst), "v"(l.voff), "s"(c.rs), "s"(src) : "memory");
        }
    }
}
template <int B, int E> DI void fill_range(const Ctx& c, const Loader& l) { if constexpr (B < E) { fill_chunk<B>(c, l); fill_range<B + 1, E>(c, l); } }
template <int EV> DI void loader_event(const Ctx& c0, const Loader& l0) {
    if constexpr (SC.need[EV] >= 0) asm volatile("s_waitcnt vmcnt(%0)\n\ts_barrier" :: "i"(SC.wait[EV]) : "memory");
    else asm volatile("s_barrier" ::: "memory");
    if constexpr (SC.fb[EV] < SC.fe[EV]) {
        Ctx c = c0; Loader l = l0;
        asm volatile("" : "+s"(c.lds0), "+s"(l.lw), "+s"(l.src[0]), "+s"(l.src[1]), "+s"(l.src[2]), "+s"(l.src[3]));
        fill_range<SC.fb[EV], SC.fe[EV]>(c, l);
    }
}
static_assert(SC.fb[EV_E1 + 1] == SC.fe[EV_B3 - 1], "no ring fills inside the attention phase");
template <int B, int E> DI void loader_events(const Ctx& c, const Loader& l) { if constexpr (B < E) { loader_event<B>(c, l); loader_events<B + 1, E>(c, l); } }

DI void ln_finish(float msum, float& mean, float& var) { mean = msum * 0.125f; }

DI void p1_compute(const Ctx& c0, int L) {
    const Ctx c = fresh(c0);
    constexpr int NB = 8;
    const int w = c.wave, lane = c.lane, tok = lane & 15, q = lane >> 4;
    const unsigned xin = WS_X + (2 * L) * XB + (c.g * 8 + 2 * w) * 20 * KB, stin = WS_ST + (2 * L) * STB + c.g * 8 * 128 * 8;
    if (L > 0) poll_group(c, L - 1, 4);
    BAR_LGKM();
    u32x2 st[2][2];
#pragma unroll
    for (int m = 0; m < 2; ++m)
#pragma unroll
        for (int i = 0; i < 2; ++i) st[m][i] = ld64(c, ((2 * q + i) * 128 + 32 * w + 16 * m + tok) * 8, stin);
    u32x4 bf[NB][2];
#pragma unroll
    for (int s = 0; s < NB; ++s)
#pragma unroll
        for (int m = 0; m < 2; ++m) bf[s][m] = ld128(c, lane * 16 + m * 20 * KB, xin + s * KB);
    const lds_cptr ring = (lds_cptr)c.lds + lane * 16;
    bf16x8 a[2][8];
#pragma unroll
    for (int t = 0; t < 8; ++t) a[0][t] = lds_frag(ring + (SC.pos[C1] + t) * KB);
    float rstd[2];
    f32x4 acc[8][2];
#pragma unroll
    for (int m = 0; m < 2; ++m) {
        const float m0 = __uint_as_float(st[m][0][0]), m1 = __uint_as_float(st[m][1][0]);
        const float mean = sum_q(m0 + m1) * 0.125f;
        const float d0 = m0 - mean, d1 = m1 - mean;
        const float M2 = sum_q(__uint_as_float(st[m][0][1]) + __uint_as_float(st[m][1][1]) + 80.f * (d0 * d0 + d1 * d1));
        rstd[m] = __builtin_amdgcn_rsqf(M2 * (1.f / DM) + LN_EPS);
#pragma unroll
        for (int t = 0; t < 8; ++t) { const f32x4 cs = *(const LAS f32x4*)((lds_cptr)c.lds + L_CSQ + (t * 16 + 4 * q) * 4); acc[t][m] = cs * (-mean); }
    }
#pragma unroll
    for (int s = 0; s < 20; ++s) {
        if ((s & 1) && s + 1 < 20) BAR_LGKM();
        if (s + 1 < 20) {
#pragma unroll
            for (int t = 0; t < 8; ++t) a[(s + 1) & 1][t] = lds_frag(ring + (SC.pos[C1 + (s + 1) / 2] + ((s + 1) & 1) * 8 + t) * KB);
        }
#pragma unroll
        for (int t = 0; t < 8; ++t)
#pragma unroll
            for (int m = 0; m < 2; ++m) acc[t][m] = MFMA16(a[s & 1][t], as_frag(bf[s % NB][m]), acc[t][m]);
        if (s + NB < 20) {
#pragma unroll
            for (int m = 0; m < 2; ++m) bf[s % NB][m] = ld128(c, lane * 16 + m * 20 * KB, xin + (s + NB) * KB);
        }
        SBAR();
    }
    const unsigned kv = WS_KV + (L * 256 + c.g * 16 + c.j) * KVB;
    const int hd = q >> 1, hf = q & 1;
#pragma unroll
    for (int m = 0; m < 2; ++m) {
        const int key = 16 * m + tok; const float r = rstd[m];
        u32x4 pr[3];
#pragma unroll
        for (int p = 0; p < 3; ++p) {
            const f32x4 x = acc[2 * p][m] * r, y = acc[2 * p + 1][m] * r;
            pr[p] = (u32x4){pk_bf16(x[0], x[1]), pk_bf16(x[2], x[3]), pk_bf16(y[0], y[1]), pk_bf16(y[2], y[3])};
        }
        const f32x4 x6 = acc[6][m] * r, x7 = acc[7][m] * r;
        const u32x2 t6 = {pk_bf16(x6[0], x6[1]), pk_bf16(x6[2], x6[3])};
        u32x2 t7 = {pk_bf16(x7[0], x7[1]), pk_bf16(x7[2], x7[3])};
        if (q == 2) t7 = (u32x2){0x00003f80u, 0u};
        if (q == 3) t7 = (u32x2){0u, 0u};
        *(LAS u32x4*)(c.lds + L_QM + hd * 4096 + w * 1024 + hf * 512 + key * 16) = pr[0];
        *(LAS u32x4*)(c.lds + L_KA + hd * 12288 + (8 + w) * 1024 + hf * 512 + key * 16) = pr[1];
        *(LAS u32x4*)(c.lds + L_VA + hd * 12288 + ((8 + w) * 32 + key) * 32 + hf * 16) = pr[2];
        if (q < 2) *(LAS u32x2*)(c.lds + L_QT + w * 512 + q * 256 + key * 8) = t6;
        else *(LAS u32x2*)(c.lds + L_KT + (8 + w) * 512 + (q - 2) * 256 + key * 8) = t6;
        *(LAS u32x2*)(c.lds + L_VT + ((8 + w) * 32 + key) * 32 + q * 8) = t7;
        st128(c, pr[1], hd * 4096 + w * 1024 + hf * 512 + key * 16, kv);
        st128(c, pr[2], 10240 + hd * 4096 + (w * 32 + key) * 32 + hf * 16, kv);
        if (q >= 2) st64(c, t6, 8192 + w * 512 + (q - 2) * 256 + key * 8, kv);
        st64(c, t7, 18432 + (w * 32 + key) * 32 + q * 8, kv);
    }
    BAR_ALL();
    publish(c, L, 0);
}

DI void p2_attention(const Ctx& c0, int L) {
    const Ctx c = fresh(c0);
    const int lane = c.lane, hh = c.wave >> 2, w = c.wave & 3, g = c.g;
    {
        const int gp = g - 1 - (lane & 1);
        poll(c, flag_off(L, 0, gp < 0 ? 0 : gp) + c.j * 4, lane < 2 && gp >= 0);
    }
    BAR_LGKM();
    {
        u32x4 v[6]; unsigned dst[6];
#pragma unroll
        for (int i = 0; i < 6; ++i) {
            const int pc = c.wave + 8 * i, gi = pc >= 22 ? 1 : 0, p = pc - 22 * gi, gs = g - 2 + gi;
            unsigned d;
            if (p < 8) d = L_KA + (p >> 2) * 12288 + gi * 4096 + (p & 3) * 1024;
            else if (p < 10) d = L_KT + gi * 2048 + (p - 8) * 1024;
            else if (p < 18) d = L_VA + ((p - 10) >> 2) * 12288 + gi * 4096 + ((p - 10) & 3) * 1024;
            else d = L_VT + gi * 4096 + (p - 18) * 1024;
            dst[i] = (pc < 44 && gs >= 0) ? d : 0xffffffffu;
            if (dst[i] != 0xffffffffu) v[i] = ld128(c, lane * 16, WS_KV + (L * 256 + gs * 16 + c.j) * KVB + p * KB);
        }
#pragma unroll
        for (int i = 0; i < 6; ++i) if (dst[i] != 0xffffffffu) *(LAS u32x4*)(c.lds + dst[i] + lane * 16) = v[i];
    }
    BAR_LGKM();
    const int h = lane >> 5, qi = lane & 31;
    const lds_cptr lb = (lds_cptr)c.lds;
    const bf16x8 qm = lds_frag(lb + L_QM + hh * 4096 + w * 1024 + lane * 16);
    u32x2 qt2 = *(const LAS u32x2*)(lb + L_QT + w * 512 + lane * 8);
    if (h != hh) qt2 = (u32x2){0u, 0u};
    const bf16x8 qt = as_frag((u32x4){qt2[0], qt2[1], 0u, 0u});
    const int lt0 = g >= 2 ? 0 : (g == 1 ? 4 : 8);
    const lds_cptr kmb = lb + L_KA + hh * 12288 + w * 1024 + lane * 16, ktb = lb + L_KT + w * 512 + lane * 8;
    f32x16 s[9];
    const float NEG = -__builtin_inff();
#pragma unroll
    for (int kt = 0; kt < 9; ++kt) {
        if (w + kt >= lt0) {
            const bf16x8 km = lds_frag(kmb + kt * 1024);
            const u32x2 k2 = *(const LAS u32x2*)(ktb + kt * 512);
            f32x16 z = {};
            z = MFMA32(km, qm, z);
            s[kt] = MFMA32(as_frag((u32x4){k2[0], k2[1], 0u, 0u}), qt, z);
        } else {
#pragma unroll
            for (int r = 0; r < 16; ++r) s[kt][r] = NEG;
        }
    }
#pragma unroll
    for (int r = 0; r < 16; ++r) {
        const bool newer = (r & 3) + 8 * (r >> 2) + 4 * h > qi;
        s[0][r] = newer ? s[0][r] : NEG;
        s[8][r] = newer ? NEG : s[8][r];
    }
    float mx = s[8][0];
#pragma unroll
    for (int kt = 0; kt < 9; ++kt)
#pragma unroll
        for (int r = 0; r < 16; r += 2) mx = fmaxf(fmaxf(mx, s[kt][r]), s[kt][r + 1]);
    mx = swap32_max(mx);
    constexpr float CS = 0.22360679774997896f * 1.4426950408889634f;
    const float nm = -mx * CS;
    const int blk = (lane >> 4) & 1;
    const lds_cptr vb = lb + (blk ? L_VT : L_VA + hh * 12288) + w * 1024 + (4 * h + ((lane & 15) >> 2)) * 32 + (lane & 3) * 8;
    f32x16 o = {};
#pragma unroll
    for (int kt = 0; kt < 9; ++kt) {
        if (w + kt >= lt0) {
            unsigned p[8];
#pragma unroll
            for (int r = 0; r < 16; r += 2)
                p[r >> 1] = pk_bf16(__builtin_amdgcn_exp2f(fmaf(s[kt][r], CS, nm)), __builtin_amdgcn_exp2f(fmaf(s[kt][r + 1], CS, nm)));
#pragma unroll
            for (int s2 = 0; s2 < 2; ++s2) {
                const s16x4 lo = __builtin_bit_cast(s16x4, __builtin_amdgcn_ds_read_tr16_b64_v4i16((LAS s16x4*)(vb + kt * 1024 + s2 * 512)));
                const s16x4 hi = __builtin_bit_cast(s16x4, __builtin_amdgcn_ds_read_tr16_b64_v4i16((LAS s16x4*)(vb + kt * 1024 + s2 * 512 + 256)));
                const bf16x8 vf = __builtin_shufflevector(lo, hi, 0, 1, 2, 3, 4, 5, 6, 7);
                o = MFMA32(vf, as_frag((u32x4){p[4 * s2], p[4 * s2 + 1], p[4 * s2 + 2], p[4 * s2 + 3]}), o);
            }
        }
    }
    auto lr = __builtin_amdgcn_permlane32_swap(__float_as_uint(o[12]), __float_as_uint(o[12]), false, false);
    const float inv = __builtin_amdgcn_rcpf(__uint_as_float(lr[0]));
    const u32x4 om = {pk_bf16(o[0] * inv, o[1] * inv), pk_bf16(o[2] * inv, o[3] * inv), pk_bf16(o[4] * inv, o[5] * inv), pk_bf16(o[6] * inv, o[7] * inv)};
    const u32x2 ot = {pk_bf16(o[8] * inv, o[9] * inv), pk_bf16(o[10] * inv, o[11] * inv)};
    const unsigned ob = WS_O + L * XB + (g * 8 + 2 * w) * 20 * KB;
    const int cm = 5 * c.j + 2 * hh + h, ct = 5 * c.j + 4;
    const unsigned tb = (qi >> 4) * 20 * KB + (qi & 15) * 16;
    st128(c, om, tb + (cm >> 2) * KB + (cm & 3) * 256, ob);
    if (h == hh) st64(c, ot, tb + (ct >> 2) * KB + (ct & 3) * 256 + hh * 8, ob);
    BAR_ALL();
    publish(c, L, 1);
}

template <int PH> DI void proj_compute(const Ctx& c0, int L) {
    const Ctx c = fresh(c0);
    constexpr int NS = PH == 3 ? 10 : 27, NC = PH == 3 ? 5 : 14, CB = PH == 3 ? C3 : C5, KS = PH == 3 ? 20 : 54, NB = 10;
    const int w = c.wave, lane = c.lane, tok = lane & 15, q = lane >> 4, ts = w >> 1, kh = w & 1, th = c.j >> 3, c8 = c.j & 7;
    const int tt0 = c.g * 8 + 4 * th + 2 * ts;
    const unsigned act = (PH == 3 ? WS_O + L * XB : WS_H + L * HB) + (tt0 * KS + NS * kh) * KB;
    const int xi = PH == 3 ? 2 * L : 2 * L + 1;
    poll_group(c, L, PH == 3 ? 1 : 3);
    BAR_LGKM();
    u32x4 bf[NB][2];
#pragma unroll
    for (int s = 0; s < NB; ++s)
#pragma unroll
        for (int m = 0; m < 2; ++m) bf[s][m] = ld128(c, lane * 16 + m * KS * KB, act + s * KB);
    const int sp = (5 * c8 + 1) >> 1, gt = (c8 & 1) ? 5 * c8 : 5 * c8 + 4;
    const unsigned sgl = (gt >> 1) * KB + (2 * (gt & 1) + (q >> 1)) * 256 + tok * 16 + (q & 1) * 8;
    const unsigned rb = WS_X + xi * XB + (tt0 + kh) * 20 * KB;
    const u32x4 r0 = ld128(c, lane * 16, rb + sp * KB), r1 = ld128(c, lane * 16, rb + (sp + 1) * KB);
    const u32x2 r2 = ld64(c, sgl, rb);
    const lds_cptr ring = (lds_cptr)c.lds + lane * 16;
    bf16x8 a[2][10];
#pragma unroll
    for (int i = 0; i < 10; ++i) a[0][i] = lds_frag(ring + SC.pos[CB] * KB + kh * 10 * KB + i * KB);
    f32x4 acc[5][2];
#pragma unroll
    for (int t = 0; t < 5; ++t) { acc[t][0] = (f32x4){0.f, 0.f, 0.f, 0.f}; acc[t][1] = acc[t][0]; }
#pragma unroll
    for (int ch = 0; ch < NC; ++ch) {
        if (ch + 1 < NC) {
            BAR_LGKM();
            const int nu = (2 * (ch + 1) + 1 < NS) ? 2 : 1;
#pragma unroll
            for (int i = 0; i < 5 * nu; ++i) a[(ch + 1) & 1][i] = lds_frag(ring + SC.pos[CB + ch + 1] * KB + kh * nu * 5 * KB + i * KB);
        }
#pragma unroll
        for (int u = 0; u < 2; ++u) {
            const int s = 2 * ch + u;
            if (s < NS) {
#pragma unroll
                for (int t = 0; t < 5; ++t)
#pragma unroll
                    for (int m = 0; m < 2; ++m) acc[t][m] = MFMA16(a[ch & 1][u * 5 + t], as_frag(bf[s % NB][m]), acc[t][m]);
                if (s + NB < NS) {
#pragma unroll
                    for (int m = 0; m < 2; ++m) bf[s % NB][m] = ld128(c, lane * 16 + m * KS * KB, act + (s + NB) * KB);
                }
            }
        }
        SBAR();
    }
    {
        lds_ptr xw = c.lds + L_XCH + w * 5 * KB + lane * 16;
#pragma unroll
        for (int t = 0; t < 5; ++t) {
            f32x4 a0 = acc[t][0], a1 = acc[t][1];
            asm volatile("" : "+v"(a0), "+v"(a1));
            *(LAS f32x4*)(xw + t * KB) = kh ? a0 : a1;
            acc[t][0] = kh ? a1 : a0;
        }
    }
    BAR_LGKM();
    float v[20];
    {
        const lds_cptr xr = (lds_cptr)c.lds + L_XCH + (w ^ 1) * 5 * KB + lane * 16;
#pragma unroll
        for (int t = 0; t < 5; ++t) {
            const f32x4 o = *(const LAS f32x4*)(xr + t * KB), mine = acc[t][0];
#pragma unroll
            for (int i = 0; i < 4; ++i) v[4 * t + i] = mine[i] + o[i];
        }
    }
    const unsigned rr[10] = {r0[0], r0[1], r0[2], r0[3], r1[0], r1[1], r1[2], r1[3], r2[0], r2[1]};
    unsigned pk[10];
#pragma unroll
    for (int i = 0; i < 10; ++i) pk[i] = pk_bf16(v[2 * i] + bf_lo(rr[i]), v[2 * i + 1] + bf_hi(rr[i]));
    if (PH == 5 && L == 1) {
        u16* orow_ = c.out + (size_t)((tt0 + kh) * 16 + tok) * DM;
        *(u32x4*)(orow_ + 32 * sp + 8 * q) = (u32x4){pk[0], pk[1], pk[2], pk[3]};
        *(u32x4*)(orow_ + 32 * (sp + 1) + 8 * q) = (u32x4){pk[4], pk[5], pk[6], pk[7]};
        *(u32x2*)(orow_ + 16 * gt + 4 * q) = (u32x2){pk[8], pk[9]};
        BAR_LGKM();
    } else {
        const unsigned ob = WS_X + (xi + 1) * XB + (tt0 + kh) * 20 * KB;
        st128(c, (u32x4){pk[0], pk[1], pk[2], pk[3]}, lane * 16, ob + sp * KB);
        st128(c, (u32x4){pk[4], pk[5], pk[6], pk[7]}, lane * 16, ob + (sp + 1) * KB);
        st64(c, (u32x2){pk[8], pk[9]}, sgl, ob);
        float sum = 0.f;
#pragma unroll
        for (int i = 0; i < 10; ++i) sum += bf_lo(pk[i]) + bf_hi(pk[i]);
        const float mean = sum_q(sum) * (1.f / 80.f);
        float m2 = 0.f;
#pragma unroll
        for (int i = 0; i < 10; ++i) { const float d0 = bf_lo(pk[i]) - mean, d1 = bf_hi(pk[i]) - mean; m2 += d0 * d0 + d1 * d1; }
        m2 = sum_q(m2);
        if (q == 0) st64(c, (u32x2){__float_as_uint(mean), __float_as_uint(m2)}, ((c.g * 8 + c8) * 128 + 64 * th + 32 * ts + 16 * kh + tok) * 8, WS_ST + (xi + 1) * STB);
        BAR_ALL();
        publish(c, L, PH == 3 ? 2 : 4);
    }
}

DI void p4_compute(const Ctx& c0, int L) {
    const Ctx c = fresh(c0);
    constexpr int NB = 12;
    const int w = c.wave, lane = c.lane, tok = lane & 31, h = lane >> 5;
    const unsigned xin = WS_X + (2 * L + 1) * XB + (c.g * 8 + 2 * w) * 20 * KB, stin = WS_ST + (2 * L + 1) * STB + c.g * 8 * 128 * 8;
    poll_group(c, L, 2);
    BAR_LGKM();
    u32x2 st[4];
#pragma unroll
    for (int i = 0; i < 4; ++i) st[i] = ld64(c, ((4 * h + i) * 128 + 32 * w + tok) * 8, stin);
    const unsigned vo = (tok >> 4) * 20 * KB + h * 256 + (tok & 15) * 16;
    u32x4 bf[NB];
#pragma unroll
    for (int s = 0; s < NB; ++s) bf[s] = ld128(c, vo + (s & 1) * 512, xin + (s >> 1) * KB);
    const lds_cptr ring = (lds_cptr)c.lds + lane * 16;
    bf16x8 a[2][7];
#pragma unroll
    for (int t = 0; t < 7; ++t) a[0][t] = lds_frag(ring + (SC.pos[C4] + t) * KB);
    float ms = 0.f;
#pragma unroll
    for (int i = 0; i < 4; ++i) ms += __uint_as_float(st[i][0]);
    const float mean = swap32_sum(ms) * 0.125f;
    float M2 = 0.f;
#pragma unroll
    for (int i = 0; i < 4; ++i) { const float d = __uint_as_float(st[i][0]) - mean; M2 += __uint_as_float(st[i][1]) + 80.f * d * d; }
    const float var = swap32_sum(M2) * (1.f / DM) + LN_EPS, rstd = __builtin_amdgcn_rsqf(var);
    f32x16 acc[7];
#pragma unroll
    for (int t = 0; t < 7; ++t)
#pragma unroll
        for (int gq = 0; gq < 4; ++gq) {
            const f32x4 cs = *(const LAS f32x4*)((lds_cptr)c.lds + L_CS13 + (t * 32 + 8 * gq + 4 * h) * 4);
#pragma unroll
            for (int i = 0; i < 4; ++i) acc[t][4 * gq + i] = cs[i] * (-mean);
        }
#pragma unroll
    for (int s = 0; s < 40; ++s) {
        if ((s & 1) && s + 1 < 40) BAR_LGKM();
        if (s + 1 < 40) {
#pragma unroll
            for (int t = 0; t < 7; ++t) a[(s + 1) & 1][t] = lds_frag(ring + (SC.pos[C4 + (s + 1) / 2] + ((s + 1) & 1) * 7 + t) * KB);
        }
#pragma unroll
        for (int t = 0; t < 7; ++t) acc[t] = MFMA32(a[s & 1][t], as_frag(bf[s % NB]), acc[t]);
        if (s + NB < 40) bf[s % NB] = ld128(c, vo + ((s + NB) & 1) * 512, xin + ((s + NB) >> 1) * KB);
        SBAR();
    }
    const float gl = -1.4426950408889634f * rstd;
    const int f0 = f_first(c.j), nt = (c.j & 3) == 3 ? 6 : 7;
    const unsigned hb = WS_H + L * HB + (c.g * 8 + 2 * w) * 54 * KB;
#pragma unroll
    for (int t = 0; t < 7; ++t) {
        float hv[8];
#pragma unroll
        for (int r = 0; r < 8; ++r) {
            const float ag = acc[t][r], au = acc[t][r + 8];
            const float e = __builtin_amdgcn_exp2f(ag * gl);
            hv[r] = (ag * au) * __builtin_amdgcn_rcpf(fmaf(e, var, var));
        }
        const int f = f0 + t;
        if (t < nt) st128(c, (u32x4){pk_bf16(hv[0], hv[1]), pk_bf16(hv[2], hv[3]), pk_bf16(hv[4], hv[5]), pk_bf16(hv[6], hv[7])},
                          (tok >> 4) * 54 * KB + h * 256 + (tok & 15) * 16, hb + (f >> 1) * KB + (f & 1) * 512);
    }
    BAR_ALL();
    publish(c, L, 3);
}

__global__ __launch_bounds__(512, 2) void main_kernel(MainArgs a) {
    extern __shared__ __attribute__((aligned(16))) char smem[];
    Ctx c;
    c.ws = a.ws; c.out = a.out;
    c.rs = __builtin_amdgcn_make_buffer_rsrc(a.ws, 0, (int)WS_END, 0x00020000);
    c.lds = (lds_ptr)smem; c.lds0 = (unsigned)(uintptr_t)c.lds;
    { const int b = blockIdx.x, xcd = b & 7, idx = b >> 3; c.g = xcd * 2 + (idx >> 4); c.j = idx & 15; }
    c.wave = __builtin_amdgcn_readfirstlane(threadIdx.x >> 6); c.lane = threadIdx.x & 63;
    Loader l; l.lw = c.wave - 4; l.voff = c.lane * 16;
    l.src[0] = WS_WQ + c.j * 160 * KB; l.src[1] = WS_WO + (c.j & 7) * 100 * KB; l.src[2] = WS_W13 + c.j * 280 * KB; l.src[3] = WS_W2 + (c.j & 7) * 270 * KB;
    if (c.wave >= 4) fill_range<0, SC.f0>(c, l);
    else {
        const int t = threadIdx.x;
        if (t < 128) *(LAS float*)(c.lds + L_CSQ + t * 4) = *(const float*)(a.ws + WS_CSQ + (c.j * 128 + t) * 4);
        if (t < 224) *(LAS float*)(c.lds + L_CS13 + t * 4) = *(const float*)(a.ws + WS_CS13 + (c.j * 224 + t) * 4);
    }
    for (int L = 0; L < 2; ++L) {
        if (c.wave < 4) p1_compute(c, L); else loader_events<EV_B1, EV_E1 + 1>(c, l);
        if (c.wave >= 4) { }
        p2_attention(c, L);
        if (c.wave < 4) { proj_compute<3>(c, L); p4_compute(c, L); proj_compute<5>(c, L); }
        else loader_events<EV_B3, NEV>(c, l);
    }
    asm volatile("s_waitcnt vmcnt(0)" ::: "memory");
}

extern "C" void kernel_launch(void* const* d_in, const int* in_sizes, int n_in, void* d_out, int out_size, void* d_ws, size_t ws_size, hipStream_t stream) {
    PrepArgs p;
    p.x = (const u16*)d_in[0]; p.wqkv = (const u16*)d_in[1]; p.wo = (const u16*)d_in[2];
    p.w1 = (const u16*)d_in[4]; p.w3 = (const u16*)d_in[5]; p.w2 = (const u16*)d_in[6]; p.ws = (char*)d_ws;
    prep_kernel<<<448, 256, 0, stream>>>(p);
    MainArgs m; m.ws = (char*)d_ws; m.out = (u16*)d_out;
    (void)hipFuncSetAttribute((const void*)main_kernel, hipFuncAttributeMaxDynamicSharedMemorySize, (int)LDS_BYTES);
    main_kernel<<<256, 512, LDS_BYTES, stream>>>(m);
}
```

```cpp
#include <hip/hip_runtime.h>
#include <stdint.h>

#define LAS __attribute__((address_space(3)))
#define GAS __attribute__((address_space(1)))
#define DI __device__ __forceinline__
typedef unsigned short u16;
typedef short bf16x8 __attribute__((ext_vector_type(8)));
typedef short s16x4 __attribute__((ext_vector_type(4)));
typedef float f32x2 __attribute__((ext_vector_type(2)));
typedef float f32x4 __attribute__((ext_vector_type(4)));
typedef float f32x16 __attribute__((ext_vector_type(16)));
typedef unsigned u32x2 __attribute__((ext_vector_type(2)));
typedef unsigned u32x4 __attribute__((ext_vector_type(4)));
typedef __bf16 bf16x2_t __attribute__((ext_vector_type(2)));
typedef GAS unsigned gu32;
#define RLX_AGENT __ATOMIC_RELAXED, __HIP_MEMORY_SCOPE_AGENT
#define AUX_LD 16
#define NB5 10
constexpr int AUX_SC1 = 16;
constexpr unsigned OOB = 0x7ffffff0u;

constexpr int SEQ = 2048, DM = 640, FF = 1728;
constexpr float LN_EPS = 1e-5f;

constexpr unsigned KB = 1024;
constexpr unsigned WS_FLAGS = 0;
constexpr unsigned WS_TMO = 24 * KB;
constexpr unsigned WS_CSQ = 32 * KB;
constexpr unsigned WS_CS13 = 192 * KB;
constexpr unsigned WS_WQ = 768 * KB;
constexpr unsigned WS_WO = WS_WQ + 16 * 160 * KB;
constexpr unsigned WS_W13 = WS_WO + 8 * 100 * KB;
constexpr unsigned WS_W2 = WS_W13 + 16 * 280 * KB;
constexpr unsigned XB = 128 * 20 * KB;
constexpr unsigned WS_X = WS_W2 + 8 * 270 * KB;
constexpr unsigned STB = 128 * KB;
constexpr unsigned WS_ST = WS_X + 4 * XB;
constexpr unsigned KVB = 22 * KB;
constexpr unsigned WS_KV = WS_ST + 4 * STB;
constexpr unsigned WS_O = WS_KV + 2 * 256 * KVB;
constexpr unsigned HB = 128 * 54 * KB;
constexpr unsigned WS_H = WS_O + 2 * XB;
constexpr unsigned WS_END = WS_H + 2 * HB;

constexpr unsigned RING_KB = 80;
constexpr unsigned L_ATT = RING_KB * KB;
constexpr unsigned L_KA = L_ATT, L_KBB = L_KA + 12288, L_KT = L_KBB + 12288;
constexpr unsigned L_VA = L_KT + 6144, L_VB = L_VA + 12288, L_VT = L_VB + 12288 + 128;
constexpr unsigned L_QM = L_VT + 12288, L_QT = L_QM + 8192;
constexpr unsigned L_CSQ = L_QT + 2048, L_CS13 = L_CSQ + 512, L_END = L_CS13 + 896;
constexpr unsigned L_XCH = L_ATT;
constexpr unsigned LDS_BYTES = 160 * KB;
static_assert(L_END <= LDS_BYTES, "LDS map");
static_assert((L_VT - L_VA) % 256 == 128 && (L_VT - L_VB) % 256 == 128, "V banks");

constexpr int NCH = 49;
constexpr int C1 = 0, C3 = 10, C4 = 15, C5 = 35;
constexpr int ch_size(int n) { return n < C3 ? 16 : n < C4 ? 20 : n < C5 ? 14 : n < NCH - 1 ? 20 : 10; }
constexpr int ch_phase(int n) { return n < C3 ? 0 : n < C4 ? 1 : n < C5 ? 2 : 3; }
constexpr int ch_src(int n) { return n < C3 ? n * 16 : n < C4 ? (n - C3) * 20 : n < C5 ? (n - C4) * 14 : (n - C5) * 20; }
constexpr int NEV = 10 + 4 + 5 + 2 + 20 + 1 + 14 + 2;
constexpr int EV_B1 = 0, EV_E1 = 10, EV_B3 = 14, EV_X3 = 19, EV_B4 = 21, EV_E6 = 41, EV_B5 = 42, EV_X5 = 56;
struct Sched { int pos[NCH]; int need[NEV]; int cons[NEV]; int wait[NEV]; int fb[NEV]; int fe[NEV]; int f0; bool ok; };
constexpr int ch_cnt(int n, int lw) { int p = (n & 1) * 2, c = 0; for (int i = 0; i < ch_size(n); ++i) if ((i & 3) == ((lw + p) & 3)) ++c; return c; }
constexpr Sched make_sched() {
    Sched s{};
    int p = 0;
    for (int n = 0; n < NCH; ++n) { if (p + ch_size(n) > (int)RING_KB) p = 0; s.pos[n] = p; p += ch_size(n); }
    for (int e = 0; e < NEV; ++e) { s.need[e] = -1; s.cons[e] = 0; }
    for (int c = 0; c < 10; ++c) { s.need[EV_B1 + c] = C1 + c; s.cons[EV_B1 + c] = C1 + c; }
    for (int e = EV_E1; e < EV_B3; ++e) s.cons[e] = C3;
    for (int c = 0; c < 5; ++c) { s.need[EV_B3 + c] = C3 + c; s.cons[EV_B3 + c] = C3 + c; }
    for (int e = EV_X3; e < EV_B4; ++e) s.cons[e] = C4;
    for (int c = 0; c < 20; ++c) { s.need[EV_B4 + c] = C4 + c; s.cons[EV_B4 + c] = C4 + c; }
    s.cons[EV_E6] = C5;
    for (int c = 0; c < 14; ++c) { s.need[EV_B5 + c] = C5 + c; s.cons[EV_B5 + c] = C5 + c; }
    for (int e = EV_X5; e < NEV; ++e) s.cons[e] = NCH;
    int f0 = 0; { int q = 0; while (f0 < NCH && s.pos[f0] == q) { q += ch_size(f0); ++f0; } }
    s.f0 = f0; s.ok = true;
    int f = f0;
    for (int e = 0; e < NEV; ++e) {
        s.wait[e] = 63;
        if (s.need[e] >= 0) {
            if (s.need[e] >= f) s.ok = false;
            int k = 63;
            for (int lw = 0; lw < 4; ++lw) { int c = 0; for (int m = s.need[e] + 1; m < f; ++m) c += ch_cnt(m % NCH, lw); if (c < k) k = c; }
            s.wait[e] = k;
        }
        s.fb[e] = f;
        while (f < NCH + f0) {
            const int m = f % NCH, lo = s.pos[m], hi = lo + ch_size(m);
            bool free_ = true;
            for (int n = s.cons[e]; n < f; ++n) { const int q = n % NCH, a = s.pos[q], b = a + ch_size(q); if (a < hi && lo < b) free_ = false; }
            if (!free_) break;
            ++f;
        }
        s.fe[e] = f;
    }
    if (f != NCH + f0) s.ok = false;
    return s;
}
constexpr Sched SC = make_sched();
static_assert(SC.ok, "ring schedule");

DI unsigned pk_bf16(float lo, float hi) { f32x2 v = {lo, hi}; bf16x2_t b = __builtin_convertvector(v, bf16x2_t); return __builtin_bit_cast(unsigned, b); }
DI float bf_lo(unsigned u) { return __uint_as_float(u << 16); }
DI float bf_hi(unsigned u) { return __uint_as_float(u & 0xffff0000u); }
DI float bf2f(u16 v) { return __uint_as_float((unsigned)v << 16); }
DI bf16x8 as_frag(u32x4 v) { return __builtin_bit_cast(bf16x8, v); }
#define MFMA16(a, b, c) __builtin_amdgcn_mfma_f32_16x16x32_bf16((a), (b), (c), 0, 0, 0)
#define MFMA32(a, b, c) __builtin_amdgcn_mfma_f32_32x32x16_bf16((a), (b), (c), 0, 0, 0)
#define BAR_LGKM() asm volatile("s_waitcnt lgkmcnt(0)\n\ts_barrier" ::: "memory")
#define BAR_ALL() asm volatile("s_waitcnt vmcnt(0) lgkmcnt(0)\n\ts_barrier" ::: "memory")
#define SBAR() __builtin_amdgcn_sched_barrier(0)
typedef const LAS char* lds_cptr;
typedef LAS char* lds_ptr;
DI bf16x8 lds_frag(lds_cptr p) { return *(const LAS bf16x8*)p; }
struct Ring { lds_cptr lo, hi; };
DI Ring make_ring(lds_cptr base) { Ring r; r.lo = base; unsigned h = (unsigned)(uintptr_t)base + 40960u; asm volatile("" : "+v"(h)); r.hi = (lds_cptr)(uintptr_t)h; return r; }
DI bf16x8 ring_frag(const Ring& r, unsigned off) { return off < 40960u ? lds_frag(r.lo + off) : lds_frag(r.hi + (off - 40960u)); }
#define SGB(mask, n) __builtin_amdgcn_sched_group_barrier((mask), (n), 0)
constexpr int SG_MFMA = 0x8, SG_DSR = 0x100, SG_VMR = 0x20;
DI float swap32_max(float m) { auto r = __builtin_amdgcn_permlane32_swap(__float_as_uint(m), __float_as_uint(m), false, false); return fmaxf(__uint_as_float(r[0]), __uint_as_float(r[1])); }
DI float swap32_sum(float m) { auto r = __builtin_amdgcn_permlane32_swap(__float_as_uint(m), __float_as_uint(m), false, false); return __uint_as_float(r[0]) + __uint_as_float(r[1]); }
DI float swap16_sum(float m) { auto r = __builtin_amdgcn_permlane16_swap(__float_as_uint(m), __float_as_uint(m), false, false); return __uint_as_float(r[0]) + __uint_as_float(r[1]); }
DI float sum_q(float v) { return swap32_sum(swap16_sum(v)); }

struct PrepArgs { const u16* x; const u16* wqkv; const u16* wo; const u16* w1; const u16* w3; const u16* w2; char* ws; };

DI int outfeat(int c8, int t, int r) {
    const int q = r >> 2, i = r & 3;
    if (t == 4) { const int gt = (c8 & 1) ? 5 * c8 : 5 * c8 + 4; return 16 * gt + r; }
    const int s = ((5 * c8 + 1) >> 1) + (t >> 1);
    return 32 * s + 8 * q + 4 * (t & 1) + i;
}
DI int qkvcol(int j, int t, int r) {
    const int q = r >> 2, i = r & 3;
    if (t < 6) return (t >> 1) * DM + (2 * j + (q >> 1)) * 20 + 8 * (q & 1) + 4 * (t & 1) + i;
    if (t == 6) return (q >> 1) * DM + (2 * j + (q & 1)) * 20 + 16 + i;
    return q < 2 ? 2 * DM + (2 * j + q) * 20 + 16 + i : -1;
}
DI int orow(int s, int q, int e) {
    const int c = 4 * s + q, j = c / 5, i = c % 5;
    if (i < 4) { const int h = i & 1; return (2 * j + (i >> 1)) * 20 + (e < 4 ? 4 * h + e : 8 + 4 * h + e - 4); }
    return (2 * j + (e >> 2)) * 20 + 16 + (e & 3);
}
DI int hrow(int s, int q, int e) { const int f = 2 * s + (q >> 1), h = q & 1; return 16 * f + (e < 4 ? 4 * h + e : 8 + 4 * h + e - 4); }
DI int f_first(int j) { return 27 * (j >> 2) + 7 * (j & 3); }

DI s16x4 tr_read(lds_cptr p) { return __builtin_bit_cast(s16x4, __builtin_amdgcn_ds_read_tr16_b64_v4i16((LAS s16x4*)p)); }
DI u32x4 tr_frag(lds_cptr p, int row4) { const s16x4 lo = tr_read(p), hi = tr_read(p + row4); return __builtin_bit_cast(u32x4, __builtin_shufflevector(lo, hi, 0, 1, 2, 3, 4, 5, 6, 7)); }
DI float frag_sum(u32x4 v) { float s = 0.f;
#pragma unroll
    for (int e = 0; e < 4; ++e) s += bf_lo(v[e]) + bf_hi(v[e]);
    return s; }
constexpr int PREP_WQ = 80, PREP_W13 = 160, PREP_WO = 40, PREP_W2 = 108, PREP_X = 128;
constexpr int PREP_UNITS = PREP_WQ + PREP_W13 + PREP_WO + PREP_W2 + PREP_X;

__global__ __launch_bounds__(256) void prep_kernel(PrepArgs a) {
    __shared__ __attribute__((aligned(16))) char img[32 * 976];
    __shared__ float red[4][64];
    int u = blockIdx.x;
    const int tid = threadIdx.x, wave = __builtin_amdgcn_readfirstlane(tid >> 6), lane = tid & 63;
    if (u < 160 && tid < 32) ((unsigned*)(a.ws + WS_FLAGS))[u * 32 + tid] = 0u;
    if (u == 160 && tid < 32) ((unsigned*)(a.ws + WS_TMO))[tid] = 0u;
    const lds_ptr L = (lds_ptr)img;
    const int q = lane >> 4, qq = (lane & 15) >> 2, p = lane & 3;
    if (u < PREP_WQ) {
        const int s = u >> 2, jq = u & 3;
        u32x4 v[8];
#pragma unroll
        for (int i = 0; i < 8; ++i) { const int c = tid + 256 * i, row = c / 60, rem = c % 60, part = rem / 20, cc = rem % 20;
            if (c < 1920) v[i] = *(const u32x4*)(a.wqkv + (size_t)(32 * s + row) * (3 * DM) + part * DM + 160 * jq + 8 * cc); }
#pragma unroll
        for (int i = 0; i < 8; ++i) { const int c = tid + 256 * i, row = c / 60, rem = c % 60;
            if (c < 1920) *(LAS u32x4*)(L + row * 976 + rem * 16) = v[i]; }
        __syncthreads();
        const int j = 4 * jq + wave;
        u32x4 f8[8];
#pragma unroll
        for (int t = 0; t < 8; ++t) {
            int col;
            if (t < 6) col = (t >> 1) * 160 + (2 * wave + (p >> 1)) * 20 + 8 * (p & 1) + 4 * (t & 1);
            else if (t == 6) col = (p >> 1) * 160 + (2 * wave + (p & 1)) * 20 + 16;
            else col = 2 * 160 + (2 * wave + (p & 1)) * 20 + 16;
            f8[t] = tr_frag((lds_cptr)L + (8 * q + qq) * 976 + col * 2, 4 * 976);
        }
        if ((lane & 15) >= 8) f8[7] = (u32x4){0u, 0u, 0u, 0u};
        float cs[8];
#pragma unroll
        for (int t = 0; t < 8; ++t) { *(u32x4*)(a.ws + WS_WQ + j * 160 * KB + (s * 8 + t) * KB + lane * 16) = f8[t]; cs[t] = sum_q(frag_sum(f8[t])); }
        const float c0 = q == 0 ? cs[0] : q == 1 ? cs[2] : q == 2 ? cs[4] : cs[6], c1 = q == 0 ? cs[1] : q == 1 ? cs[3] : q == 2 ? cs[5] : cs[7];
        float* dst = (float*)(a.ws + WS_CSQ) + (j * 20 + s) * 128 + q * 32 + (lane & 15);
        dst[0] = c0; dst[16] = c1;
    } else if ((u -= PREP_WQ) < PREP_W13) {
        const int s = u >> 2, jq = u & 3;
        u32x4 v[7];
#pragma unroll
        for (int i = 0; i < 7; ++i) { const int c = tid + 256 * i, mat = c / 864, rem = c % 864, row = rem / 54, cc = rem % 54;
            if (c < 1728) v[i] = *(const u32x4*)((mat ? a.w3 : a.w1) + (size_t)(16 * s + row) * FF + 432 * jq + 8 * cc); }
#pragma unroll
        for (int i = 0; i < 7; ++i) { const int c = tid + 256 * i, mat = c / 864, rem = c % 864, row = rem / 54, cc = rem % 54;
            if (c < 1728) *(LAS u32x4*)(L + (mat * 16 + row) * 880 + cc * 16) = v[i]; }
        __syncthreads();
        const int j = 4 * jq + wave, blk = q & 1, h = q >> 1;
        u32x4 f7[7];
#pragma unroll
        for (int t = 0; t < 7; ++t) {
            const int fl = (wave == 3 && t == 6) ? 26 : 7 * wave + t;
            f7[t] = tr_frag((lds_cptr)L + (blk * 16 + 8 * h + qq) * 880 + (16 * fl + 4 * p) * 2, 4 * 880);
        }
        float cs[7];
#pragma unroll
        for (int t = 0; t < 7; ++t) { *(u32x4*)(a.ws + WS_W13 + j * 280 * KB + (s * 7 + t) * KB + lane * 16) = f7[t]; cs[t] = swap32_sum(frag_sum(f7[t])); }
        float* dst = (float*)(a.ws + WS_CS13) + (j * 40 + s) * 224 + (lane & 31);
#pragma unroll
        for (int t = 0; t < 4; ++t) { const float v_ = h ? cs[t < 3 ? 4 + t : 6] : cs[t]; dst[(h ? (t < 3 ? 4 + t : 6) : t) * 32] = v_; }
    } else if ((u -= PREP_W13) < PREP_WO + PREP_W2) {
        const bool isO = u < PREP_WO; if (!isO) u -= PREP_WO;
        const int s = u >> 1, ch = u & 1;
        const u16* W = isO ? a.wo : a.w2;
        u32x4 v[5];
#pragma unroll
        for (int i = 0; i < 5; ++i) { const int c = tid + 256 * i, row = c / 40, cc = c % 40;
            const int kr = isO ? orow(s, row >> 3, row & 7) : hrow(s, row >> 3, row & 7);
            v[i] = *(const u32x4*)(W + (size_t)kr * DM + 320 * ch + 8 * cc); }
#pragma unroll
        for (int i = 0; i < 5; ++i) { const int c = tid + 256 * i, row = c / 40, cc = c % 40; *(LAS u32x4*)(L + row * 656 + cc * 16) = v[i]; }
        __syncthreads();
        const int c8 = 4 * ch + wave, ns = isO ? 10 : 27, kh = s / ns, sl = s % ns;
        const unsigned base = isO ? WS_WO + c8 * 100 * KB : WS_W2 + c8 * 270 * KB;
        const int off = (!isO && sl == 26) ? 13 * 20 + kh * 5 : (sl >> 1) * 20 + (kh * 2 + (sl & 1)) * 5;
        u32x4 f5[5];
#pragma unroll
        for (int t = 0; t < 5; ++t) f5[t] = tr_frag((lds_cptr)L + (8 * q + qq) * 656 + (outfeat(c8, t, 4 * p) - 320 * ch) * 2, 4 * 656);
#pragma unroll
        for (int t = 0; t < 5; ++t) *(u32x4*)(a.ws + base + (off + t) * KB + lane * 16) = f5[t];
    } else {
        const int tt = u - (PREP_WO + PREP_W2), tok = lane & 15;
        u32x4 v[5]; float sum = 0.f;
#pragma unroll
        for (int i = 0; i < 5; ++i) v[i] = *(const u32x4*)(a.x + (size_t)(16 * tt + tok) * DM + 32 * (wave * 5 + i) + 8 * q);
#pragma unroll
        for (int i = 0; i < 5; ++i) { *(u32x4*)(a.ws + WS_X + (tt * 20 + wave * 5 + i) * KB + lane * 16) = v[i]; sum += frag_sum(v[i]); }
        sum = sum_q(sum);
        red[wave][lane] = sum; __syncthreads();
        const float mean = ((red[0][tok] + red[1][tok]) + (red[2][tok] + red[3][tok])) * (1.f / DM);
        __syncthreads();
        float m2 = 0.f;
#pragma unroll
        for (int i = 0; i < 5; ++i)
#pragma unroll
            for (int e = 0; e < 4; ++e) { const float d0 = bf_lo(v[i][e]) - mean, d1 = bf_hi(v[i][e]) - mean; m2 += d0 * d0 + d1 * d1; }
        m2 = sum_q(m2);
        red[wave][lane] = m2; __syncthreads();
        if (tid < 128) {
            const int c8 = tid >> 4, tk = tid & 15;
            const float mean_t = __shfl(mean, tk);
            const float tot = (red[0][tk] + red[1][tk]) + (red[2][tk] + red[3][tk]);
            const int g = tt >> 3, tl = (tt & 7) * 16 + tk;
            *(f32x2*)(a.ws + WS_ST + ((g * 8 + c8) * 128 + tl) * 8) = (f32x2){mean_t, c8 == 0 ? tot : 0.f};
        }
    }
}

struct MainArgs { char* ws; u16* out; };

struct Ctx {
    __amdgpu_buffer_rsrc_t rs;
    char* ws; u16* out;
    lds_ptr lds; unsigned lds0;
    int g, j, wave, lane;
};

DI Ctx fresh(const Ctx& c0) {
    Ctx c = c0;
    asm volatile("" : "+v"(c.lane)); asm volatile("" : "+s"(c.g)); asm volatile("" : "+s"(c.j)); asm volatile("" : "+s"(c.wave)); asm volatile("" : "+s"(c.lds0));
    return c;
}
DI u32x4 ld128(const Ctx& c, unsigned voff, unsigned soff) { return __builtin_amdgcn_raw_buffer_load_b128(c.rs, voff, soff, AUX_LD); }
DI u32x2 ld64(const Ctx& c, unsigned voff, unsigned soff) { return __builtin_amdgcn_raw_buffer_load_b64(c.rs, voff, soff, AUX_SC1); }
DI float ld32_plain(const Ctx& c, unsigned voff, unsigned soff) { return __uint_as_float(__builtin_amdgcn_raw_buffer_load_b32(c.rs, voff, soff, 0)); }
DI void st128(const Ctx& c, u32x4 v, unsigned voff, unsigned soff) { __builtin_amdgcn_raw_buffer_store_b128(v, c.rs, voff, soff, AUX_SC1); }
DI void st64(const Ctx& c, u32x2 v, unsigned voff, unsigned soff) { __builtin_amdgcn_raw_buffer_store_b64(v, c.rs, voff, soff, AUX_SC1); }

#define STAMP(c, L, i) do { } while (0)
#define TICK(v) do { } while (0)
#define TICKSTORE(c, L, i, v) do { } while (0)
DI unsigned flag_off(int L, int ph, int g) { return WS_FLAGS + ((L * 5 + ph) * 16 + g) * 128; }
DI void publish(const Ctx& c, int L, int ph) {
    if (c.wave == 0 && c.lane == 0) __hip_atomic_store((gu32*)(c.ws + flag_off(L, ph, c.g)) + c.j, 1u, RLX_AGENT);
}
DI void poll(const Ctx& c, unsigned off, bool use) {
    if (c.wave != 0) return;
    for (unsigned spins = 0;; ++spins) {
        const unsigned v = use ? __hip_atomic_load((gu32*)(c.ws + off), RLX_AGENT) : 1u;
        if (__all(v == 1u)) break;
        if (spins > (1u << 20)) { if (c.lane == 0) __hip_atomic_store((gu32*)(c.ws + WS_TMO), 1u, RLX_AGENT); break; }
        __builtin_amdgcn_s_sleep(1);
    }
}
DI void poll_group(const Ctx& c, int L, int ph) { poll(c, flag_off(L, ph, c.g) + (c.lane & 15) * 4, true); }

struct Loader { unsigned voff; unsigned src[4]; int lw; };
template <int M> DI void fill_chunk(const Ctx& c, const Loader& l) {
    constexpr int n = M % NCH, size = ch_size(n), p = (n & 1) * 2;
    const unsigned rot = (unsigned)((l.lw + p) & 3);
#pragma unroll
    for (int i0 = 0; i0 < (size + 3) / 4; ++i0) {
        const unsigned i = 4 * i0 + rot;
        if (4 * i0 + 3 < size || i < (unsigned)size) {
            const unsigned dst = c.lds0 + (SC.pos[n] + 4 * i0) * KB + rot * KB, src = l.src[ch_phase(n)] + (ch_src(n) + 4 * i0) * KB + rot * KB;
            asm volatile("s_mov_b32 m0, %0\n\ts_nop 0\n\tbuffer_load_dwordx4 %1, %2, %3 offen lds" :: "s"(dst), "v"(l.voff), "s"(c.rs), "s"(src) : "memory");
        }
    }
}
template <int B, int E> DI void fill_range(const Ctx& c, const Loader& l) { if constexpr (B < E) { fill_chunk<B>(c, l); fill_range<B + 1, E>(c, l); } }
template <int EV> DI void loader_event(const Ctx& c0, const Loader& l0) {
    if constexpr (SC.need[EV] >= 0) asm volatile("s_waitcnt vmcnt(%0)\n\ts_barrier" :: "i"(SC.wait[EV]) : "memory");
    if constexpr (SC.need[EV] < 0) asm volatile("s_barrier" ::: "memory");
    if constexpr (SC.fb[EV] < SC.fe[EV]) {
        Ctx c = c0; Loader l = l0;
        asm volatile("" : "+s"(c.lds0)); asm volatile("" : "+s"(l.lw));
        asm volatile("" : "+s"(l.src[0])); asm volatile("" : "+s"(l.src[1])); asm volatile("" : "+s"(l.src[2])); asm volatile("" : "+s"(l.src[3]));
        fill_range<SC.fb[EV], SC.fe[EV]>(c, l);
    }
}
static_assert(SC.fb[EV_E1 + 1] == SC.fe[EV_B3 - 1], "no ring fills inside the attention phase");
template <int B, int E> DI void loader_events(const Ctx& c, const Loader& l) { if constexpr (B < E) { loader_event<B>(c, l); loader_events<B + 1, E>(c, l); } }

DI void ln_finish(float msum, float& mean, float& var) { mean = msum * 0.125f; }

DI void p1_compute(const Ctx& c0, int L) {
    const Ctx c = fresh(c0);
    constexpr int NB = 8;
    const int w = c.wave, lane = c.lane, tok = lane & 15, q = lane >> 4;
    const unsigned xin = WS_X + (2 * L) * XB + (c.g * 8 + 2 * w) * 20 * KB, stin = WS_ST + (2 * L) * STB + c.g * 8 * 128 * 8;
    STAMP(c, L, 0);
    if (L > 0) poll_group(c, L - 1, 4);
    STAMP(c, L, 1);
    BAR_LGKM();
    STAMP(c, L, 2);
    u32x4 bf[NB][2];
#pragma unroll
    for (int s = 0; s < NB; ++s)
#pragma unroll
        for (int m = 0; m < 2; ++m) bf[s][m] = ld128(c, lane * 16 + m * 20 * KB, xin + s * KB);
    const Ring ring = make_ring((lds_cptr)c.lds + lane * 16);
    bf16x8 a[2][8];
#pragma unroll
    for (int t = 0; t < 8; ++t) a[0][t] = ring_frag(ring, (SC.pos[C1] + t) * KB);
    u32x2 st[2][2];
#pragma unroll
    for (int m = 0; m < 2; ++m)
#pragma unroll
        for (int i = 0; i < 2; ++i) st[m][i] = ld64(c, ((2 * q + i) * 128 + 32 * w + 16 * m + tok) * 8, stin);
    if (L == 0 && w < 2) {
        float p[20];
#pragma unroll
        for (int i = 0; i < 20; ++i) p[i] = ld32_plain(c, (w * 64 + lane) * 4, WS_CSQ + (c.j * 20 + i) * 512);
        float t = 0.f;
#pragma unroll
        for (int i = 0; i < 20; ++i) t += p[i];
        *(LAS float*)(c.lds + L_CSQ + (w * 64 + lane) * 4) = t;
    }
    f32x4 acc[8][2];
#pragma unroll
    for (int t = 0; t < 8; ++t) { acc[t][0] = (f32x4){0.f, 0.f, 0.f, 0.f}; acc[t][1] = acc[t][0]; }
#pragma unroll
    for (int s = 0; s < 20; ++s) {
        if ((s & 1) && s + 1 < 20) BAR_LGKM();
        if (s + 1 < 20) {
#pragma unroll
            for (int t = 0; t < 8; ++t) a[(s + 1) & 1][t] = ring_frag(ring, (SC.pos[C1 + (s + 1) / 2] + ((s + 1) & 1) * 8 + t) * KB);
        }
#pragma unroll
        for (int t = 0; t < 8; ++t)
#pragma unroll
            for (int m = 0; m < 2; ++m) acc[t][m] = MFMA16(a[s & 1][t], as_frag(bf[s % NB][m]), acc[t][m]);
        if (s + NB < 20) {
#pragma unroll
            for (int m = 0; m < 2; ++m) bf[s % NB][m] = ld128(c, lane * 16 + m * 20 * KB, xin + (s + NB) * KB);
        }
        if (s + 1 < 20) {
#pragma unroll
            for (int t = 0; t < 8; ++t) { SGB(SG_MFMA, 1); SGB(SG_DSR, 1); }
        }
        SGB(SG_MFMA, 16);
        SGB(SG_VMR, 2);
        SBAR();
    }
    STAMP(c, L, 3);
    const unsigned kv = WS_KV + (L * 256 + c.g * 16 + c.j) * KVB;
    const int hd = q >> 1, hf = q & 1;
    f32x4 cs[8];
#pragma unroll
    for (int t = 0; t < 8; ++t) cs[t] = *(const LAS f32x4*)((lds_cptr)c.lds + L_CSQ + (t * 16 + 4 * q) * 4);
#pragma unroll
    for (int m = 0; m < 2; ++m) {
        const float m0 = __uint_as_float(st[m][0][0]), m1 = __uint_as_float(st[m][1][0]);
        const float mean = sum_q(m0 + m1) * 0.125f;
        const float d0 = m0 - mean, d1 = m1 - mean;
        const float M2 = sum_q(__uint_as_float(st[m][0][1]) + __uint_as_float(st[m][1][1]) + 80.f * (d0 * d0 + d1 * d1));
        const float r = __builtin_amdgcn_rsqf(M2 * (1.f / DM) + LN_EPS);
#pragma unroll
        for (int t = 0; t < 8; ++t) acc[t][m] -= cs[t] * mean;
        const int key = 16 * m + tok;
        u32x4 pr[3];
#pragma unroll
        for (int p = 0; p < 3; ++p) {
            const f32x4 x = acc[2 * p][m] * r, y = acc[2 * p + 1][m] * r;
            pr[p] = (u32x4){pk_bf16(x[0], x[1]), pk_bf16(x[2], x[3]), pk_bf16(y[0], y[1]), pk_bf16(y[2], y[3])};
        }
        const f32x4 x6 = acc[6][m] * r, x7 = acc[7][m] * r;
        const u32x2 t6 = {pk_bf16(x6[0], x6[1]), pk_bf16(x6[2], x6[3])};
        u32x2 t7 = {pk_bf16(x7[0], x7[1]), pk_bf16(x7[2], x7[3])};
        if (q == 2) t7 = (u32x2){0x00003f80u, 0u};
        if (q == 3) t7 = (u32x2){0u, 0u};
        *(LAS u32x4*)(c.lds + L_QM + hd * 4096 + w * 1024 + hf * 512 + key * 16) = pr[0];
        *(LAS u32x4*)(c.lds + L_KA + hd * 12288 + (8 + w) * 1024 + hf * 512 + key * 16) = pr[1];
        *(LAS u32x4*)(c.lds + L_VA + hd * 12288 + ((8 + w) * 32 + key) * 32 + hf * 16) = pr[2];
        if (q < 2) *(LAS u32x2*)(c.lds + L_QT + w * 512 + q * 256 + key * 8) = t6;
        else *(LAS u32x2*)(c.lds + L_KT + (8 + w) * 512 + (q - 2) * 256 + key * 8) = t6;
        *(LAS u32x2*)(c.lds + L_VT + ((8 + w) * 32 + key) * 32 + q * 8) = t7;
        st128(c, pr[1], hd * 4096 + w * 1024 + hf * 512 + key * 16, kv);
        st128(c, pr[2], 10240 + hd * 4096 + (w * 32 + key) * 32 + hf * 16, kv);
        if (q >= 2) st64(c, t6, 8192 + w * 512 + (q - 2) * 256 + key * 8, kv);
        st64(c, t7, 18432 + (w * 32 + key) * 32 + q * 8, kv);
    }
    STAMP(c, L, 4);
    BAR_ALL();
    publish(c, L, 0);
    STAMP(c, L, 5);
}

template <bool PARTIAL> DI f32x16 attn_body(const Ctx& c, const bf16x8 qm, const bf16x8 qt, const f32x16 b_old, const f32x16 b_diag, int lt0) {
    const int lane = c.lane, hh = c.wave >> 2, w = c.wave & 3, h = lane >> 5;
    const lds_cptr lb = (lds_cptr)c.lds;
    const lds_cptr kmb = lb + L_KA + hh * 12288 + w * 1024 + lane * 16, ktb = lb + L_KT + w * 512 + lane * 8;
    const lds_cptr vb = lb + (((lane >> 4) & 1) ? L_VT : L_VA + hh * 12288) + w * 1024 + (4 * h + ((lane & 15) >> 2)) * 32 + (lane & 3) * 8;
    const float NEG = -__builtin_inff();
    f32x16 s[9];
    bf16x8 km[3]; u32x2 k2[3];
#pragma unroll
    for (int kt = 0; kt < 2; ++kt) { km[kt] = lds_frag(kmb + kt * 1024); k2[kt] = *(const LAS u32x2*)(ktb + kt * 512); }
#pragma unroll
    for (int kt = 0; kt < 9; ++kt) {
        if (kt + 2 < 9) { km[(kt + 2) % 3] = lds_frag(kmb + (kt + 2) * 1024); k2[(kt + 2) % 3] = *(const LAS u32x2*)(ktb + (kt + 2) * 512); }
        f32x16 z;
        if (kt == 0) z = b_old; else if (kt == 8) z = b_diag;
        else {
#pragma unroll
            for (int r = 0; r < 16; ++r) z[r] = 0.f;
        }
        if (PARTIAL && kt < 8) {
            const bool dead = w + kt < lt0;
#pragma unroll
            for (int r = 0; r < 16; ++r) z[r] = dead ? NEG : z[r];
        }
        z = MFMA32(km[kt % 3], qm, z);
        s[kt] = MFMA32(as_frag((u32x4){k2[kt % 3][0], k2[kt % 3][1], 0u, 0u}), qt, z);
        if (!PARTIAL) { if (kt + 2 < 9) SGB(SG_DSR, 2); SGB(SG_MFMA, 2); }
    }
    SBAR();
    float mx[4] = {s[8][0], s[8][1], s[8][2], s[8][3]};
#pragma unroll
    for (int kt = 0; kt < 9; ++kt)
#pragma unroll
        for (int r = 0; r < 16; r += 2) mx[(r >> 1) & 3] = fmaxf(fmaxf(mx[(r >> 1) & 3], s[kt][r]), s[kt][r + 1]);
    const float m = swap32_max(fmaxf(fmaxf(mx[0], mx[1]), fmaxf(mx[2], mx[3])));
    constexpr float CS = 0.22360679774997896f * 1.4426950408889634f;
    const f32x2 cs2 = {CS, CS}, nm2 = {-m * CS, -m * CS};
    f32x16 o = {};
    s16x4 vf[2][4];
#pragma unroll
    for (int i = 0; i < 4; ++i) vf[0][i] = tr_read(vb + i * 256);
    SBAR();
#pragma unroll
    for (int kt = 0; kt < 9; ++kt) {
        if (kt + 1 < 9) {
#pragma unroll
            for (int i = 0; i < 4; ++i) vf[(kt + 1) & 1][i] = tr_read(vb + (kt + 1) * 1024 + i * 256);
        }
        unsigned p[8];
#pragma unroll
        for (int r = 0; r < 16; r += 2) {
            const f32x2 t = __builtin_elementwise_fma((f32x2){s[kt][r], s[kt][r + 1]}, cs2, nm2);
            p[r >> 1] = pk_bf16(__builtin_amdgcn_exp2f(t[0]), __builtin_amdgcn_exp2f(t[1]));
        }
#pragma unroll
        for (int s2 = 0; s2 < 2; ++s2)
            o = MFMA32(__builtin_shufflevector(vf[kt & 1][2 * s2], vf[kt & 1][2 * s2 + 1], 0, 1, 2, 3, 4, 5, 6, 7),
                       as_frag((u32x4){p[4 * s2], p[4 * s2 + 1], p[4 * s2 + 2], p[4 * s2 + 3]}), o);
        SBAR();
    }
    return o;
}

DI void p2_attention(const Ctx& c0, int L) {
    const Ctx c = fresh(c0);
    const int lane = c.lane, hh = c.wave >> 2, w = c.wave & 3, g = c.g;
    {
        const int gp = g - 1 - (lane & 1);
        poll(c, flag_off(L, 0, gp < 0 ? 0 : gp) + c.j * 4, lane < 2 && gp >= 0);
    }
    BAR_LGKM();
    STAMP(c, L, 6);
    u32x4 v[6]; unsigned dst[6];
#pragma unroll
    for (int i = 0; i < 6; ++i) {
        const int pc = c.wave + 8 * i, gi = pc >= 22 ? 1 : 0, p = pc - 22 * gi, gs = g - 2 + gi;
        unsigned d;
        if (p < 8) d = L_KA + (p >> 2) * 12288 + gi * 4096 + (p & 3) * 1024;
        else if (p < 10) d = L_KT + gi * 2048 + (p - 8) * 1024;
        else if (p < 18) d = L_VA + ((p - 10) >> 2) * 12288 + gi * 4096 + ((p - 10) & 3) * 1024;
        else d = L_VT + gi * 4096 + (p - 18) * 1024;
        dst[i] = d;
        const bool live = pc < 44 && gs >= 0;
        v[i] = ld128(c, live ? lane * 16 : OOB, WS_KV + (L * 256 + (live ? gs : 0) * 16 + c.j) * KVB + (live ? p : 0) * KB);
    }
    const int h = lane >> 5, qi = lane & 31;
    const lds_cptr lb = (lds_cptr)c.lds;
    const bf16x8 qm = lds_frag(lb + L_QM + hh * 4096 + w * 1024 + lane * 16);
    u32x2 qt2 = *(const LAS u32x2*)(lb + L_QT + w * 512 + lane * 8);
    if (h != hh) qt2 = (u32x2){0u, 0u};
    const bf16x8 qt = as_frag((u32x4){qt2[0], qt2[1], 0u, 0u});
    f32x16 b_old, b_diag;
    {
        const float NEG = -__builtin_inff();
        const int d = qi - 4 * h;
#pragma unroll
        for (int r = 0; r < 16; ++r) {
            const bool newer = (r & 3) + 8 * (r >> 2) > d;
            b_old[r] = newer ? 0.f : NEG;
            b_diag[r] = newer ? NEG : 0.f;
        }
        asm volatile("" : "+v"(b_old), "+v"(b_diag));
    }
#pragma unroll
    for (int i = 0; i < 6; ++i) if (c.wave + 8 * i < 44) *(LAS u32x4*)(c.lds + dst[i] + lane * 16) = v[i];
    BAR_LGKM();
    STAMP(c, L, 7);
    const f32x16 o = g >= 2 ? attn_body<false>(c, qm, qt, b_old, b_diag, 0) : attn_body<true>(c, qm, qt, b_old, b_diag, g == 1 ? 4 : 8);
    STAMP(c, L, 8);
    auto lr = __builtin_amdgcn_permlane32_swap(__float_as_uint(o[12]), __float_as_uint(o[12]), false, false);
    const float inv = __builtin_amdgcn_rcpf(__uint_as_float(lr[0]));
    const u32x4 om = {pk_bf16(o[0] * inv, o[1] * inv), pk_bf16(o[2] * inv, o[3] * inv), pk_bf16(o[4] * inv, o[5] * inv), pk_bf16(o[6] * inv, o[7] * inv)};
    const u32x2 ot = {pk_bf16(o[8] * inv, o[9] * inv), pk_bf16(o[10] * inv, o[11] * inv)};
    const unsigned ob = WS_O + L * XB + (g * 8 + 2 * w) * 20 * KB;
    const int cm = 5 * c.j + 2 * hh + h, ct = 5 * c.j + 4;
    const unsigned tb = (qi >> 4) * 20 * KB + (qi & 15) * 16;
    st128(c, om, tb + (cm >> 2) * KB + (cm & 3) * 256, ob);
    if (h == hh) st64(c, ot, tb + (ct >> 2) * KB + (ct & 3) * 256 + hh * 8, ob);
    BAR_ALL();
    publish(c, L, 1);
    STAMP(c, L, 9);
}

template <int PH> DI void proj_compute(const Ctx& c0, int L) {
    const Ctx c = fresh(c0);
    constexpr int NS = PH == 3 ? 10 : 27, NC = PH == 3 ? 5 : 14, CB = PH == 3 ? C3 : C5, KS = PH == 3 ? 20 : 54, NB = PH == 3 ? 10 : NB5;
    const int w = c.wave, lane = c.lane, tok = lane & 15, q = lane >> 4, ts = w >> 1, kh = w & 1, th = c.j >> 3, c8 = c.j & 7;
    const int tt0 = c.g * 8 + 4 * th + 2 * ts;
    const unsigned act = (PH == 3 ? WS_O + L * XB : WS_H + L * HB) + (tt0 * KS + NS * kh) * KB;
    const int xi = PH == 3 ? 2 * L : 2 * L + 1;
    poll_group(c, L, PH == 3 ? 1 : 3);
    STAMP(c, L, PH == 3 ? 10 : 20);
    BAR_LGKM();
    STAMP(c, L, PH == 3 ? 11 : 21);
    u32x4 bf[NB][2];
#pragma unroll
    for (int s = 0; s < NB; ++s)
#pragma unroll
        for (int m = 0; m < 2; ++m) bf[s][m] = ld128(c, lane * 16 + m * KS * KB, act + s * KB);
    const int sp = (5 * c8 + 1) >> 1, gt = (c8 & 1) ? 5 * c8 : 5 * c8 + 4;
    const unsigned sgl = (gt >> 1) * KB + (2 * (gt & 1) + (q >> 1)) * 256 + tok * 16 + (q & 1) * 8;
    const unsigned rb = WS_X + xi * XB + (tt0 + kh) * 20 * KB;
    const u32x4 r0 = ld128(c, lane * 16, rb + sp * KB), r1 = ld128(c, lane * 16, rb + (sp + 1) * KB);
    const u32x2 r2 = ld64(c, sgl, rb);
    const Ring ring2 = make_ring((lds_cptr)c.lds + lane * 16 + kh * 10 * KB), ring1 = make_ring((lds_cptr)c.lds + lane * 16 + kh * 5 * KB);
    bf16x8 a[2][10];
#pragma unroll
    for (int i = 0; i < 10; ++i) a[0][i] = ring_frag(ring2, (SC.pos[CB] + i) * KB);
    f32x4 acc[5][2];
#pragma unroll
    for (int t = 0; t < 5; ++t) { acc[t][0] = (f32x4){0.f, 0.f, 0.f, 0.f}; acc[t][1] = acc[t][0]; }
#pragma unroll
    for (int ch = 0; ch < NC; ++ch) {
        if (ch + 1 < NC) {
            BAR_LGKM();
            const int nu = (2 * (ch + 1) + 1 < NS) ? 2 : 1;
#pragma unroll
            for (int i = 0; i < 5 * nu; ++i) a[(ch + 1) & 1][i] = ring_frag(nu == 2 ? ring2 : ring1, (SC.pos[CB + ch + 1] + i) * KB);
        }
#pragma unroll
        for (int u = 0; u < 2; ++u) {
            const int s = 2 * ch + u;
            if (s < NS) {
#pragma unroll
                for (int t = 0; t < 5; ++t)
#pragma unroll
                    for (int m = 0; m < 2; ++m) acc[t][m] = MFMA16(a[ch & 1][u * 5 + t], as_frag(bf[s % NB][m]), acc[t][m]);
                if (s + NB < NS) {
#pragma unroll
                    for (int m = 0; m < 2; ++m) bf[s % NB][m] = ld128(c, lane * 16 + m * KS * KB, act + (s + NB) * KB);
                }
            }
        }
        if (ch + 1 < NC) {
#pragma unroll
            for (int i = 0; i < 10; ++i) { SGB(SG_MFMA, 1); SGB(SG_DSR, 1); }
        }
        SGB(SG_MFMA, 20);
        SGB(SG_VMR, 4);
        SBAR();
    }
    STAMP(c, L, PH == 3 ? 12 : 22);
    {
        lds_ptr xw = c.lds + L_XCH + w * 5 * KB + lane * 16;
#pragma unroll
        for (int t = 0; t < 5; ++t) {
            f32x4 a0 = acc[t][0], a1 = acc[t][1];
            asm volatile("" : "+v"(a0), "+v"(a1));
            *(LAS f32x4*)(xw + t * KB) = kh ? a0 : a1;
            acc[t][0] = kh ? a1 : a0;
        }
    }
    BAR_LGKM();
    float v[20];
    {
        const lds_cptr xr = (lds_cptr)c.lds + L_XCH + (w ^ 1) * 5 * KB + lane * 16;
#pragma unroll
        for (int t = 0; t < 5; ++t) {
            const f32x4 o = *(const LAS f32x4*)(xr + t * KB), mine = acc[t][0];
#pragma unroll
            for (int i = 0; i < 4; ++i) v[4 * t + i] = mine[i] + o[i];
        }
    }
    const unsigned rr[10] = {r0[0], r0[1], r0[2], r0[3], r1[0], r1[1], r1[2], r1[3], r2[0], r2[1]};
    unsigned pk[10];
#pragma unroll
    for (int i = 0; i < 10; ++i) pk[i] = pk_bf16(v[2 * i] + bf_lo(rr[i]), v[2 * i + 1] + bf_hi(rr[i]));
    if (PH == 5 && L == 1) {
        u16* orow_ = c.out + (size_t)((tt0 + kh) * 16 + tok) * DM;
        *(u32x4*)(orow_ + 32 * sp + 8 * q) = (u32x4){pk[0], pk[1], pk[2], pk[3]};
        *(u32x4*)(orow_ + 32 * (sp + 1) + 8 * q) = (u32x4){pk[4], pk[5], pk[6], pk[7]};
        *(u32x2*)(orow_ + 16 * gt + 4 * q) = (u32x2){pk[8], pk[9]};
        BAR_LGKM();
        STAMP(c, L, 24);
    } else {
        const unsigned ob = WS_X + (xi + 1) * XB + (tt0 + kh) * 20 * KB;
        st128(c, (u32x4){pk[0], pk[1], pk[2], pk[3]}, lane * 16, ob + sp * KB);
        st128(c, (u32x4){pk[4], pk[5], pk[6], pk[7]}, lane * 16, ob + (sp + 1) * KB);
        st64(c, (u32x2){pk[8], pk[9]}, sgl, ob);
        float sum = 0.f;
#pragma unroll
        for (int i = 0; i < 10; ++i) sum += bf_lo(pk[i]) + bf_hi(pk[i]);
        const float mean = sum_q(sum) * (1.f / 80.f);
        float m2 = 0.f;
#pragma unroll
        for (int i = 0; i < 10; ++i) { const float d0 = bf_lo(pk[i]) - mean, d1 = bf_hi(pk[i]) - mean; m2 += d0 * d0 + d1 * d1; }
        m2 = sum_q(m2);
        if (q == 0) st64(c, (u32x2){__float_as_uint(mean), __float_as_uint(m2)}, ((c.g * 8 + c8) * 128 + 64 * th + 32 * ts + 16 * kh + tok) * 8, WS_ST + (xi + 1) * STB);
        STAMP(c, L, PH == 3 ? 13 : 23);
        BAR_ALL();
        publish(c, L, PH == 3 ? 2 : 4);
        STAMP(c, L, PH == 3 ? 14 : 24);
    }
}

DI void p4_compute(const Ctx& c0, int L) {
    const Ctx c = fresh(c0);
    constexpr int NB = 12;
    const int w = c.wave, lane = c.lane, tok = lane & 31, h = lane >> 5;
    const unsigned xin = WS_X + (2 * L + 1) * XB + (c.g * 8 + 2 * w) * 20 * KB, stin = WS_ST + (2 * L + 1) * STB + c.g * 8 * 128 * 8;
    poll_group(c, L, 2);
    STAMP(c, L, 15);
    BAR_LGKM();
    STAMP(c, L, 16);
    unsigned long long tk[8] = {};
    TICK(tk[0]);
    const unsigned vo = (tok >> 4) * 20 * KB + h * 256 + (tok & 15) * 16;
    u32x4 bf[NB];
#pragma unroll
    for (int s = 0; s < NB; ++s) bf[s] = ld128(c, vo + (s & 1) * 512, xin + (s >> 1) * KB);
    const Ring ring = make_ring((lds_cptr)c.lds + lane * 16);
    bf16x8 a[2][7];
#pragma unroll
    for (int t = 0; t < 7; ++t) a[0][t] = ring_frag(ring, (SC.pos[C4] + t) * KB);
    u32x2 st[4];
#pragma unroll
    for (int i = 0; i < 4; ++i) st[i] = ld64(c, ((4 * h + i) * 128 + 32 * w + tok) * 8, stin);
    f32x16 acc[7];
#pragma unroll
    for (int t = 0; t < 7; ++t)
#pragma unroll
        for (int r = 0; r < 16; ++r) acc[t][r] = 0.f;
#pragma unroll
    for (int s = 0; s < 40; ++s) {
        if ((s & 1) && s + 1 < 40) BAR_LGKM();
        if (s == 1) TICK(tk[1]); if (s == 3) TICK(tk[2]); if (s == 11) TICK(tk[3]); if (s == 21) TICK(tk[4]); if (s == 31) TICK(tk[5]); if (s == 37) TICK(tk[6]);
        if (s + 1 < 40) {
#pragma unroll
            for (int t = 0; t < 7; ++t) a[(s + 1) & 1][t] = ring_frag(ring, (SC.pos[C4 + (s + 1) / 2] + ((s + 1) & 1) * 7 + t) * KB);
        }
#pragma unroll
        for (int t = 0; t < 7; ++t) acc[t] = MFMA32(a[s & 1][t], as_frag(bf[s % NB]), acc[t]);
        if (s + NB < 40) bf[s % NB] = ld128(c, vo + ((s + NB) & 1) * 512, xin + ((s + NB) >> 1) * KB);
        if (s + 1 < 40) {
#pragma unroll
            for (int t = 0; t < 3; ++t) { SGB(SG_MFMA, 1); SGB(SG_DSR, 2); }
            SGB(SG_MFMA, 1); SGB(SG_DSR, 1);
        }
        SGB(SG_MFMA, 7);
        SGB(SG_VMR, 1);
        SBAR();
    }
    TICK(tk[7]);
#pragma unroll
    for (int i = 0; i < 8; ++i) TICKSTORE(c, L, i, tk[i]);
    STAMP(c, L, 17);
    float ms = 0.f;
#pragma unroll
    for (int i = 0; i < 4; ++i) ms += __uint_as_float(st[i][0]);
    const float mean = swap32_sum(ms) * 0.125f;
    float M2 = 0.f;
#pragma unroll
    for (int i = 0; i < 4; ++i) { const float d = __uint_as_float(st[i][0]) - mean; M2 += __uint_as_float(st[i][1]) + 80.f * d * d; }
    const float var = swap32_sum(M2) * (1.f / DM) + LN_EPS, rstd = __builtin_amdgcn_rsqf(var);
    const float gl = -1.4426950408889634f * rstd;
    const int f0 = f_first(c.j), nt = (c.j & 3) == 3 ? 6 : 7;
    const unsigned hb = WS_H + L * HB + (c.g * 8 + 2 * w) * 54 * KB;
#pragma unroll
    for (int t = 0; t < 7; ++t) {
        float hv[8];
#pragma unroll
        for (int gq = 0; gq < 4; ++gq) {
            const f32x4 cs = *(const LAS f32x4*)((lds_cptr)c.lds + L_CS13 + (t * 32 + 8 * gq + 4 * h) * 4);
#pragma unroll
            for (int i = 0; i < 4; ++i) acc[t][4 * gq + i] -= cs[i] * mean;
        }
#pragma unroll
        for (int r = 0; r < 8; ++r) {
            const float ag = acc[t][r], au = acc[t][r + 8];
            const float e = __builtin_amdgcn_exp2f(ag * gl);
            hv[r] = (ag * au) * __builtin_amdgcn_rcpf(fmaf(e, var, var));
        }
        const int f = f0 + t;
        if (t < nt) st128(c, (u32x4){pk_bf16(hv[0], hv[1]), pk_bf16(hv[2], hv[3]), pk_bf16(hv[4], hv[5]), pk_bf16(hv[6], hv[7])},
                          (tok >> 4) * 54 * KB + h * 256 + (tok & 15) * 16, hb + (f >> 1) * KB + (f & 1) * 512);
    }
    STAMP(c, L, 18);
    BAR_ALL();
    publish(c, L, 3);
    STAMP(c, L, 19);
}

DI void cs13_reduce(const Ctx& c0) {
    const Ctx c = fresh(c0);
    const int t = (c.wave - 4) * 64 + c.lane;
    if (t < 224) {
        float p[40];
#pragma unroll
        for (int i = 0; i < 40; ++i) p[i] = ld32_plain(c, t * 4, WS_CS13 + (c.j * 40 + i) * 896);
        float sum = 0.f;
#pragma unroll
        for (int i = 0; i < 40; ++i) sum += p[i];
        *(LAS float*)(c.lds + L_CS13 + t * 4) = sum;
    }
}

__global__ __launch_bounds__(512, 2) void main_kernel(MainArgs a) {
    extern __shared__ __attribute__((aligned(16))) char smem[];
    Ctx c;
    c.ws = a.ws; c.out = a.out;
    c.rs = __builtin_amdgcn_make_buffer_rsrc(a.ws, 0, (int)WS_END, 0x00020000);
    c.lds = (lds_ptr)smem; c.lds0 = (unsigned)(uintptr_t)c.lds;
    { const int b = blockIdx.x, xcd = b & 7, idx = b >> 3; c.g = xcd * 2 + (idx >> 4); c.j = idx & 15; }
    c.wave = __builtin_amdgcn_readfirstlane(threadIdx.x >> 6); c.lane = threadIdx.x & 63;
    Loader l; l.lw = c.wave - 4; l.voff = c.lane * 16;
    l.src[0] = WS_WQ + c.j * 160 * KB; l.src[1] = WS_WO + (c.j & 7) * 100 * KB; l.src[2] = WS_W13 + c.j * 280 * KB; l.src[3] = WS_W2 + (c.j & 7) * 270 * KB;
    if (c.wave >= 4) fill_range<0, SC.f0>(c, l);
    for (int L = 0; L < 2; ++L) {
        if (c.wave < 4) p1_compute(c, L); else loader_events<EV_B1, EV_E1 + 1>(c, l);
        if (L == 0 && c.wave >= 4) cs13_reduce(c);
        p2_attention(c, L);
        if (c.wave < 4) { proj_compute<3>(c, L); p4_compute(c, L); proj_compute<5>(c, L); }
        else loader_events<EV_B3, NEV>(c, l);
    }
    asm volatile("s_waitcnt vmcnt(0)" ::: "memory");
}

extern "C" void kernel_launch(void* const* d_in, const int* in_sizes, int n_in, void* d_out, int out_size, void* d_ws, size_t ws_size, hipStream_t stream) {
    PrepArgs p;
    p.x = (const u16*)d_in[0]; p.wqkv = (const u16*)d_in[1]; p.wo = (const u16*)d_in[2];
    p.w1 = (const u16*)d_in[4]; p.w3 = (const u16*)d_in[5]; p.w2 = (const u16*)d_in[6]; p.ws = (char*)d_ws;
    prep_kernel<<<PREP_UNITS, 256, 0, stream>>>(p);
    MainArgs m; m.ws = (char*)d_ws; m.out = (u16*)d_out;
    (void)hipFuncSetAttribute((const void*)main_kernel, hipFuncAttributeMaxDynamicSharedMemorySize, (int)LDS_BYTES);
    main_kernel<<<256, 512, LDS_BYTES, stream>>>(m);
}
```

```cpp
#include <hip/hip_runtime.h>
#include <stdint.h>

#define LAS __attribute__((address_space(3)))
#define GAS __attribute__((address_space(1)))
#define DI __device__ __forceinline__
typedef unsigned short u16;
typedef short bf16x8 __attribute__((ext_vector_type(8)));
typedef short s16x4 __attribute__((ext_vector_type(4)));
typedef float f32x2 __attribute__((ext_vector_type(2)));
typedef float f32x4 __attribute__((ext_vector_type(4)));
typedef float f32x16 __attribute__((ext_vector_type(16)));
typedef unsigned u32x2 __attribute__((ext_vector_type(2)));
typedef unsigned u32x4 __attribute__((ext_vector_type(4)));
typedef __bf16 bf16x2_t __attribute__((ext_vector_type(2)));
typedef GAS unsigned gu32;
#define RLX_AGENT __ATOMIC_RELAXED, __HIP_MEMORY_SCOPE_AGENT
#define AUX_LD 16
#define POLL_SLEEP 1
#define HID_DELAY 2
#define KV_DELAY -1
#define PREP_AUX 16
#define PROJ_REV 0
#define PRIO_C 0
#define PRIO_A 0
#define AWAIT_DELAY 14
#define CANARY_DELAY 13
#define WIN0 7
#define NB4 7
#define NB1 8
#define NB5 10
constexpr int AUX_SC1 = 16;
constexpr unsigned OOB = 0x7ffffff0u;

constexpr int SEQ = 2048, DM = 640, FF = 1728;
constexpr float LN_EPS = 1e-5f;

constexpr unsigned KB = 1024;
constexpr unsigned WS_FLAGS = 0;
constexpr unsigned WS_TMO = 128 * KB;
constexpr unsigned WS_CSQ = 132 * KB, WS_CS13 = 140 * KB;

constexpr unsigned WS_WQ = 160 * KB;
constexpr unsigned WS_WO = WS_WQ + 16 * 160 * KB;
constexpr unsigned WS_W13 = WS_WO + 8 * 100 * KB;
constexpr unsigned WS_W2 = WS_W13 + 16 * 280 * KB;
constexpr unsigned XB = 128 * 20 * KB;
constexpr unsigned WS_X = WS_W2 + 8 * 270 * KB;
constexpr unsigned STB = 128 * KB;
constexpr unsigned WS_ST = WS_X + 4 * XB;
constexpr unsigned KVB = 22 * KB;
constexpr unsigned WS_KV = WS_ST + 4 * STB;
constexpr unsigned WS_O = WS_KV + 2 * 256 * KVB;
constexpr unsigned HB = 128 * 54 * KB;
constexpr unsigned WS_H = WS_O + 2 * XB;
constexpr unsigned WS_END = WS_H + 2 * HB;

constexpr unsigned RING_KB = 80;
constexpr unsigned L_ATT = RING_KB * KB;
constexpr unsigned L_KA = L_ATT, L_KBB = L_KA + 12288, L_VA = L_KBB + 12288, L_VB = L_VA + 12288, L_VT = L_VB + 12288 + 128, L_KT = L_VT + 12288;
constexpr unsigned L_QM = L_KT + 6144, L_QT = L_QM + 8192;
constexpr unsigned L_JUNK = L_QT + 2048;
constexpr unsigned L_CSQ = L_JUNK + 1024, L_CS13 = L_CSQ + 512, L_END = L_CS13 + 896;
constexpr unsigned RING2_KB = 156;
constexpr unsigned LDS_BYTES = 160 * KB;
static_assert(L_END + 4 <= LDS_BYTES && RING2_KB * KB <= L_JUNK, "LDS map");
static_assert((L_VT - L_VA) % 256 == 128 && (L_VT - L_VB) % 256 == 128, "V banks");

constexpr int NCH = 49;
constexpr int C1 = 0, C3 = 10, C4 = 15, C5 = 35;
constexpr int ch_size(int n) { return n < C3 ? 16 : n < C4 ? 20 : n < C5 ? 14 : n < NCH - 1 ? 20 : 10; }
constexpr int ch_phase(int n) { return n < C3 ? 0 : n < C4 ? 1 : n < C5 ? 2 : 3; }
constexpr int ch_src(int n) { return n < C3 ? n * 16 : n < C4 ? (n - C3) * 20 : n < C5 ? (n - C4) * 14 : (n - C5) * 20; }
constexpr int NEV = 10 + 4 + 5 + 2 + 20 + 1 + 14 + 2;
constexpr int EV_B1 = 0, EV_E1 = 10, EV_B3 = 14, EV_X3 = 19, EV_B4 = 21, EV_E6 = 41, EV_B5 = 42, EV_X5 = 56;
#define PRO_EXTRA 0
#define WO_EXT 0
#define START_C4 2
#define START_C1 11
#define F0_MAX 9
#define START_B4X 20
#define CAP_B4X 40
#define NOCLEAN 0
#define CAP_B0 0
#define CAP_B 20
#define CAP_E 0
#define CAP_X3 14
#define CAP_X5 32
struct Sched { int pos[NCH]; int need[NEV]; int cons[NEV]; int wait[NEV]; int wait_last[NEV]; int wait_first[NEV]; int fb[NEV]; int fe[NEV]; int f0; int fp; bool ok; };
constexpr bool ch_ext(int n) { return WO_EXT && n >= C3 && n < C3 + 4; }
constexpr int ch_run(int n, int lw) { const int q = ch_size(n) / 4, r = ch_size(n) % 4; return q + ((r && ((n & 1) ? lw >= 4 - r : lw < r)) ? 1 : 0); }
constexpr int ch_cnt(int n, int lw) { return ch_ext(n) ? 0 : ch_run(n, lw); }
constexpr int ch_place(const Sched& s, int f, int cons, bool low) {
    const int m = f % NCH, size = ch_size(m), lim = (low || (m >= C3 - 1 && m < C4)) ? (int)RING_KB : (int)RING2_KB;
    for (int i = 0; i + size <= lim; i += 2) {
        int lo = i;
        if (m >= C3 && m < C4) { if (i % 20) continue; lo = (i + 40) % 80; }
        if (m >= C5 && i % 20) continue;
        bool free_ = true;
        for (int n = cons; n < f; ++n) { const int q = n % NCH, a = s.pos[q], b = a + ch_size(q); if (a < lo + size && lo < b) free_ = false; }
        if (free_) return lo;
    }
    return -1;
}
constexpr Sched make_sched(const int f0) {
    Sched s{};
    for (int e = 0; e < NEV; ++e) { s.need[e] = -1; s.cons[e] = 0; }
    for (int c = 0; c < 10; ++c) { s.need[EV_B1 + c] = C1 + c; s.cons[EV_B1 + c] = C1 + c; }
    for (int e = EV_E1; e < EV_B3; ++e) s.cons[e] = C3;
    for (int c = 0; c < 5; ++c) { s.need[EV_B3 + c] = C3 + c; s.cons[EV_B3 + c] = C3 + c; }
    for (int e = EV_X3; e < EV_B4; ++e) s.cons[e] = C4;
    for (int c = 0; c < 20; ++c) { s.need[EV_B4 + c] = C4 + c; s.cons[EV_B4 + c] = C4 + c; }
    s.cons[EV_E6] = C5;
    for (int c = 0; c < 14; ++c) { s.need[EV_B5 + c] = C5 + c; s.cons[EV_B5 + c] = C5 + c; }
    for (int e = EV_X5; e < NEV; ++e) s.cons[e] = NCH;
    int fp = 0;
    for (;;) { const int p = ch_place(s, fp, 0, false); if (p < 0) break; s.pos[fp] = p; ++fp; }
    s.f0 = f0; s.fp = fp; s.ok = f0 <= fp;
    int f = f0;
    for (int e = 0; e < NEV; ++e) {
        s.wait[e] = 63;
        if (s.need[e] >= 0) {
            if (s.need[e] >= f) s.ok = false;
            int k = 63;
            for (int lw = 0; lw < 4; ++lw) { int c = 0; for (int m = s.need[e] + 1; m < f; ++m) c += ch_cnt(m % NCH, lw); if (c < k) k = c; }
            s.wait[e] = k;
            k = 63;
            for (int lw = 0; lw < 4; ++lw) { int c = 0; for (int m = s.need[e] + 1; m < f && m < NCH; ++m) c += ch_cnt(m, lw); if (c < k) k = c; }
            s.wait_last[e] = k;
            k = 63;
            const int pro = PRO_EXTRA + f0 < fp ? PRO_EXTRA + f0 : fp;
            for (int lw = 0; lw < 4; ++lw) { int c = 0; for (int m = s.need[e] + 1; m < (f > pro ? f : pro); ++m) c += ch_cnt(m % NCH, lw); if (c < k) k = c; }
            s.wait_first[e] = k;
        }
        s.fb[e] = f;
        int budget = e == EV_X3 ? CAP_X3 : e == EV_X5 ? CAP_X5 : (e == EV_E1 || e == EV_E1 + 1 || e == EV_E1 + 3) ? 0 : (e == EV_X3 + 1 || e == EV_E6 || e == EV_X5 + 1) ? CAP_E
                   : (e == EV_B1 || e == EV_B3 || e == EV_B4 || e == EV_B5) ? CAP_B0 : e == EV_E1 + 2 ? 1000 : (e >= EV_B4 + START_B4X && e < EV_E6) ? CAP_B4X : CAP_B;
        while (f < NCH + f0) {
            if (WO_EXT && f >= C3 && e < EV_E1 + 2) break;
            if (f >= C4 && e < EV_B3 + START_C4) break;
            if (f >= NCH && e < EV_B5 + START_C1) break;
            if (ch_size(f % NCH) > budget) break;
            int p = -1;
            if (f >= NCH || f < fp) {
                p = s.pos[f % NCH];
                if (f >= NCH) for (int n = s.cons[e]; n < NCH; ++n) if (s.pos[n] < p + ch_size(f - NCH) && p < s.pos[n] + ch_size(n)) p = -1;
            } else p = ch_place(s, f, s.cons[e], e < EV_B3 && f >= C3);
            if (p < 0) break;
            if (f < NCH) s.pos[f] = p;
            budget -= ch_size(f % NCH);
            ++f;
        }
        s.fe[e] = f;
    }
    if (f != NCH + f0) s.ok = false;
    return s;
}
constexpr Sched find_sched() { for (int f0 = F0_MAX; f0 >= 1; --f0) { const Sched s = make_sched(f0); if (s.ok) return s; } return make_sched(1); }
constexpr Sched SC = find_sched();
constexpr int PRO = PRO_EXTRA + SC.f0 < SC.fp ? PRO_EXTRA + SC.f0 : SC.fp;
static_assert(SC.ok, "ring schedule");

DI unsigned pk_bf16(float lo, float hi) { f32x2 v = {lo, hi}; bf16x2_t b = __builtin_convertvector(v, bf16x2_t); return __builtin_bit_cast(unsigned, b); }
DI float bf_lo(unsigned u) { return __uint_as_float(u << 16); }
DI float bf_hi(unsigned u) { return __uint_as_float(u & 0xffff0000u); }
DI float bf2f(u16 v) { return __uint_as_float((unsigned)v << 16); }
DI bf16x8 as_frag(u32x4 v) { return __builtin_bit_cast(bf16x8, v); }
DI f32x2 lo2(f32x4 v) { return (f32x2){v[0], v[1]}; }
DI f32x2 hi2(f32x4 v) { return (f32x2){v[2], v[3]}; }
DI f32x2 splat2(float x) { f32x2 r = {x, x}; asm volatile("" : "+v"(r)); return r; }
DI f32x2 fma2(f32x2 a, f32x2 b, f32x2 c) { return __builtin_elementwise_fma(a, b, c); }
constexpr unsigned SENT = 0xffffffffu;
DI unsigned clean(unsigned v) { return NOCLEAN ? v : (v < 0xfffffffeu ? v : 0xfffffffeu); }
DI u32x2 clean2(u32x2 v) { return (u32x2){clean(v[0]), clean(v[1])}; }
DI u32x4 clean4(u32x4 v) { return (u32x4){clean(v[0]), clean(v[1]), clean(v[2]), clean(v[3])}; }
DI unsigned umax3(unsigned a, unsigned b, unsigned c) { const unsigned m = a > b ? a : b; return m > c ? m : c; }
DI unsigned umax4(u32x4 v, unsigned m) { return umax3(umax3(m, v[0], v[1]), v[2], v[3]); }
DI bool missing(unsigned m) { return __any(m == SENT); }
DI unsigned chk(u32x4 v, unsigned m) { return umax3(m, v[0], v[2]); }
#define MFMA16(a, b, c) __builtin_amdgcn_mfma_f32_16x16x32_bf16((a), (b), (c), 0, 0, 0)
#define MFMA32(a, b, c) __builtin_amdgcn_mfma_f32_32x32x16_bf16((a), (b), (c), 0, 0, 0)
DI void mf16(f32x4& acc, bf16x8 a, bf16x8 b) { asm volatile("v_mfma_f32_16x16x32_bf16 %0, %1, %2, %0" : "+v"(acc) : "v"(a), "v"(b)); }
DI void mf16_first(f32x4& acc, bf16x8 a, bf16x8 b) { asm volatile("v_mfma_f32_16x16x32_bf16 %0, %1, %2, 0" : "=&v"(acc) : "v"(a), "v"(b)); }
#define MF16(first, acc, a, b) do { if (first) mf16_first(acc, a, b); else mf16(acc, a, b); } while (0)
#define MFMA_DONE() do { SBAR(); asm volatile("s_nop 7\n\ts_nop 3" ::: "memory"); SBAR(); } while (0)
#define PIN4(x) asm volatile("" : "+v"(x))
constexpr int win_upto(int s, int NB, int NS, int rate, int n0 = WIN0) {
    int n = n0;
    for (int i = 0; i <= s; ++i) { int m = n + rate; if (m > i + 1 + NB) m = i + 1 + NB; if (m > NS) m = NS; if (m > n) n = m; }
    return n;
}
template <int V> struct IC { static constexpr int value = V; };
template <int B, int E, class F> DI void static_for(F&& f) { if constexpr (B < E) { f(IC<B>{}); static_for<B + 1, E>(f); } }
#define BAR_LGKM() asm volatile("s_waitcnt lgkmcnt(0)\n\ts_barrier" ::: "memory")
#define BAR_ALL() asm volatile("s_waitcnt vmcnt(0) lgkmcnt(0)\n\ts_barrier" ::: "memory")
#define SBAR() __builtin_amdgcn_sched_barrier(0)
typedef const LAS char* lds_cptr;
typedef LAS char* lds_ptr;
DI bf16x8 lds_frag(lds_cptr p) { return *(const LAS bf16x8*)p; }
struct Ring { lds_cptr lo, hi, top; };
DI Ring make_ring(lds_cptr base) {
    Ring r; r.lo = base;
    unsigned h = (unsigned)(uintptr_t)base + 61440u, t = (unsigned)(uintptr_t)base + 122880u;
    asm volatile("" : "+v"(h)); asm volatile("" : "+v"(t));
    r.hi = (lds_cptr)(uintptr_t)h; r.top = (lds_cptr)(uintptr_t)t; return r;
}
DI bf16x8 ring_frag(const Ring& r, unsigned off) { return off < 61440u ? lds_frag(r.lo + off) : off < 122880u ? lds_frag(r.hi + (off - 61440u)) : lds_frag(r.top + (off - 122880u)); }
#define SGB(mask, n) __builtin_amdgcn_sched_group_barrier((mask), (n), 0)
constexpr int SG_MFMA = 0x8, SG_DSR = 0x100, SG_VMR = 0x20, SG_VALU = 0x2;
DI float swap32_max(float m) { auto r = __builtin_amdgcn_permlane32_swap(__float_as_uint(m), __float_as_uint(m), false, false); return fmaxf(__uint_as_float(r[0]), __uint_as_float(r[1])); }
DI float swap32_sum(float m) { auto r = __builtin_amdgcn_permlane32_swap(__float_as_uint(m), __float_as_uint(m), false, false); return __uint_as_float(r[0]) + __uint_as_float(r[1]); }
DI float swap16_sum(float m) { auto r = __builtin_amdgcn_permlane16_swap(__float_as_uint(m), __float_as_uint(m), false, false); return __uint_as_float(r[0]) + __uint_as_float(r[1]); }
DI float sum_q(float v) { return swap32_sum(swap16_sum(v)); }

struct PrepArgs { const u16* x; const u16* wqkv; const u16* wo; const u16* w1; const u16* w3; const u16* w2; char* ws; };

DI int outfeat(int c8, int t, int r) {
    const int q = r >> 2, i = r & 3;
    if (t == 4) { const int gt = (c8 & 1) ? 5 * c8 : 5 * c8 + 4; return 16 * gt + r; }
    const int s = ((5 * c8 + 1) >> 1) + (t >> 1);
    return 32 * s + 8 * q + 4 * (t & 1) + i;
}
DI int qkvcol(int j, int t, int r) {
    const int q = r >> 2, i = r & 3; const bool tail = t >= 6;
    const int part = tail ? (t == 6 ? q >> 1 : 2) : t >> 1, head = tail ? (q & 1) : (q >> 1), d = tail ? 16 + i : 8 * (q & 1) + 4 * (t & 1) + i;
    return (t == 7 && q >= 2) ? -1 : part * DM + (2 * j + head) * 20 + d;
}
DI int orow(int s, int q, int e) {
    const int c = 4 * s + q, j = c / 5, i = c % 5;
    if (i < 4) { const int h = i & 1; return (2 * j + (i >> 1)) * 20 + (e < 4 ? 4 * h + e : 8 + 4 * h + e - 4); }
    return (2 * j + (e >> 2)) * 20 + 16 + (e & 3);
}
DI int hidfeat(int j, int i, int r) {
    const int single = 16 * ((j & 1) ? 7 * j : 7 * j + 6) + r, pair = 32 * (((7 * j + 1) >> 1) + (i >> 1)) + 8 * (r >> 2) + 4 * (i & 1) + (r & 3);
    return i == 6 ? single : pair;
}

constexpr int hid_block(int s) {
    return s >= 46 ? 7 * (s - 46) + 3 : s < 16 ? 7 * (s >> 1) + 4 * (s & 1) : s < 31 ? 7 * ((s - 16) >> 1) + 4 * ((s - 16) & 1) + 1 : 7 * ((s - 31) >> 1) + 4 * ((s - 31) & 1) + 2;
}
DI int hid_step(int b) { const int jp = b / 7, r = b % 7, j = 2 * jp + (r > 3 ? 1 : 0), i = r > 3 ? r - 4 : r; return r == 3 ? 46 + jp : (i == 0 ? 0 : i == 1 ? 16 : 31) + j; }

DI s16x4 tr_read(lds_cptr p) { return __builtin_bit_cast(s16x4, __builtin_amdgcn_ds_read_tr16_b64_v4i16((LAS s16x4*)p)); }
DI u32x4 tr_frag(lds_cptr p, int row4) { const s16x4 lo = tr_read(p), hi = tr_read(p + row4); return __builtin_bit_cast(u32x4, __builtin_shufflevector(lo, hi, 0, 1, 2, 3, 4, 5, 6, 7)); }
DI float frag_sum(u32x4 v) { float s = 0.f;
#pragma unroll
    for (int e = 0; e < 4; ++e) s += bf_lo(v[e]) + bf_hi(v[e]);
    return s; }
DI void prep_st(const PrepArgs& a, unsigned off, u32x4 v) {
    const __amdgpu_buffer_rsrc_t rs = __builtin_amdgcn_make_buffer_rsrc(a.ws, 0, (int)WS_END, 0x00020000);
    __builtin_amdgcn_raw_buffer_store_b128(v, rs, off, 0, PREP_AUX);
    asm volatile("s_nop 1" :: "v"(v) : "memory");
}
constexpr int PREP_CS = 84, PREP_WQ = 80, PREP_W13 = 160, PREP_WO = 40, PREP_W2 = 108, PREP_X = 128;
constexpr int PREP_UNITS = PREP_CS + PREP_WQ + PREP_W13 + PREP_WO + PREP_W2 + PREP_X;

__global__ __launch_bounds__(256) void prep_kernel(PrepArgs a) {
    __shared__ __attribute__((aligned(16))) char img[32 * 976];
    __shared__ float red[4][64];
    int u = blockIdx.x < PREP_X ? blockIdx.x + (PREP_UNITS - PREP_X) : blockIdx.x - PREP_X;
    const int tid = threadIdx.x, wave = __builtin_amdgcn_readfirstlane(tid >> 6), lane = tid & 63;
    if (u < 128) ((unsigned*)(a.ws + WS_FLAGS))[u * 256 + tid] = 0u;
    if (u == 160 && tid < 32) ((unsigned*)(a.ws + WS_TMO))[tid] = 0u;
    const lds_ptr L = (lds_ptr)img;
    const int q = lane >> 4, qq = (lane & 15) >> 2, p = lane & 3;
    if (u < PREP_CS) {
        const int mat = u < 30 ? 0 : u < 57 ? 1 : 2, cb = 64 * (u < 30 ? u : u < 57 ? u - 30 : u - 57), N = mat ? FF : 3 * DM;
        const u16* W = mat == 0 ? a.wqkv : mat == 1 ? a.w1 : a.w3;
        const int cg = tid & 7, rg = tid >> 3;
        u32x4 v[20];
#pragma unroll
        for (int i = 0; i < 20; ++i) v[i] = *(const u32x4*)(W + (size_t)(20 * rg + i) * N + cb + 8 * cg);
        float t[8] = {};
#pragma unroll
        for (int i = 0; i < 20; ++i)
#pragma unroll
            for (int e = 0; e < 4; ++e) { t[2 * e] += bf_lo(v[i][e]); t[2 * e + 1] += bf_hi(v[i][e]); }
        LAS float* R = (LAS float*)L;
        *(LAS f32x4*)(R + rg * 68 + 8 * cg) = (f32x4){t[0], t[1], t[2], t[3]};
        *(LAS f32x4*)(R + rg * 68 + 8 * cg + 4) = (f32x4){t[4], t[5], t[6], t[7]};
        __syncthreads();
        if (tid < 64) {
            float sum = 0.f;
#pragma unroll
            for (int i = 0; i < 32; ++i) sum += R[i * 68 + tid];
            const int n = cb + tid;
            if (mat == 0) {
                const int part = n / DM, head = (n % DM) / 20, d = n % 20, hd = head & 1;
                const int t_ = d < 16 ? 2 * part + ((d >> 2) & 1) : (part < 2 ? 6 : 7), qr = d < 16 ? 2 * hd + (d >> 3) : (part < 2 ? 2 * part + hd : hd);
                ((float*)(a.ws + WS_CSQ))[(head >> 1) * 128 + t_ * 16 + 4 * qr + (d & 3)] = sum;
            } else {
                const int f = n >> 4, j = f / 7, gt = (j & 1) ? 7 * j : 7 * j + 6, e = n & 31;
                const int i = f == gt ? 6 : 2 * ((f >> 1) - ((7 * j + 1) >> 1)) + ((e >> 2) & 1), r = f == gt ? (n & 15) : 4 * (e >> 3) + (e & 3);
                ((float*)(a.ws + WS_CS13))[j * 224 + ((mat - 1) * 7 + i) * 16 + r] = sum;
            }
        }
    } else if ((u -= PREP_CS) < PREP_WQ) {
        const int s = u >> 2, jq = u & 3;
        unsigned long long k0 = 0, k1 = 0, k2 = 0, k3 = 0; (void)k0; (void)k1; (void)k2; (void)k3;
        u32x4 v[8];
#pragma unroll
        for (int i = 0; i < 8; ++i) { const int c = min(tid + 256 * i, 1919), row = c / 60, rem = c % 60, part = rem / 20, cc = rem % 20;
            v[i] = *(const u32x4*)(a.wqkv + (size_t)(32 * s + row) * (3 * DM) + part * DM + 160 * jq + 8 * cc); }
#pragma unroll
        for (int i = 0; i < 8; ++i) { const int c = min(tid + 256 * i, 1919), row = c / 60, rem = c % 60;
            *(LAS u32x4*)(L + row * 976 + rem * 16) = v[i]; }
        __syncthreads();
        const int j = 4 * jq + wave;
        u32x4 f8[8];
#pragma unroll
        for (int t = 0; t < 8; ++t) {
            int col;
            if (t < 6) col = (t >> 1) * 160 + (2 * wave + (p >> 1)) * 20 + 8 * (p & 1) + 4 * (t & 1);
            else if (t == 6) col = (p >> 1) * 160 + (2 * wave + (p & 1)) * 20 + 16;
            else col = 2 * 160 + (2 * wave + (p & 1)) * 20 + 16;
            f8[t] = tr_frag((lds_cptr)L + (8 * q + qq) * 976 + col * 2, 4 * 976);
        }
        if ((lane & 15) >= 8) f8[7] = (u32x4){0u, 0u, 0u, 0u};
#pragma unroll
        for (int t = 0; t < 8; ++t) prep_st(a, WS_WQ + j * 160 * KB + (s * 8 + t) * KB + lane * 16, f8[t]);
    } else if ((u -= PREP_WQ) < PREP_W13) {
        const int s = u >> 3, jp = u & 7;
        u32x4 v[7];
#pragma unroll
        for (int i = 0; i < 7; ++i) { const int c = tid + 256 * i, mat = c / 896, rem = c % 896, row = rem / 28, cc = rem % 28, col = min(224 * jp + 8 * cc, FF - 8);
            v[i] = *(const u32x4*)((mat ? a.w3 : a.w1) + (size_t)(32 * s + row) * FF + col); }
#pragma unroll
        for (int i = 0; i < 7; ++i) { const int c = tid + 256 * i, mat = c / 896, rem = c % 896, row = rem / 28, cc = rem % 28;
            *(LAS u32x4*)(L + (mat * 32 + row) * 464 + cc * 16) = v[i]; }
        __syncthreads();
        const int j = 2 * jp + (wave >> 1), mat = wave & 1;
        u32x4 f7[7];
#pragma unroll
        for (int t = 0; t < 7; ++t) f7[t] = tr_frag((lds_cptr)L + (mat * 32 + 8 * q + qq) * 464 + (hidfeat(j, t, 4 * p) - 224 * jp) * 2, 4 * 464);
#pragma unroll
        for (int t = 0; t < 7; ++t) prep_st(a, WS_W13 + j * 280 * KB + (s * 14 + mat * 7 + t) * KB + lane * 16, f7[t]);
    } else if ((u -= PREP_W13) < PREP_WO + PREP_W2) {
        const bool isO = u < PREP_WO; if (!isO) u -= PREP_WO;
        const int s = u >> 1, ch = u & 1;
        const u16* W = isO ? a.wo : a.w2;
        u32x4 v[5];
#pragma unroll
        for (int i = 0; i < 5; ++i) { const int c = tid + 256 * i, row = c / 40, cc = c % 40;
            const int kr = isO ? orow(s, row >> 3, row & 7) : 32 * s + row;
            v[i] = *(const u32x4*)(W + (size_t)kr * DM + 320 * ch + 8 * cc); }
#pragma unroll
        for (int i = 0; i < 5; ++i) { const int c = tid + 256 * i, row = c / 40, cc = c % 40; *(LAS u32x4*)(L + row * 656 + cc * 16) = v[i]; }
        __syncthreads();
        const int c8 = 4 * ch + wave;
        const unsigned base = isO ? WS_WO + c8 * 100 * KB : WS_W2 + c8 * 270 * KB;
        const int off = (isO ? s : hid_step(s)) * 5;
        u32x4 f5[5];
#pragma unroll
        for (int t = 0; t < 5; ++t) f5[t] = tr_frag((lds_cptr)L + (8 * q + qq) * 656 + (outfeat(c8, t, 4 * p) - 320 * ch) * 2, 4 * 656);
#pragma unroll
        for (int t = 0; t < 5; ++t) prep_st(a, base + (off + t) * KB + lane * 16, f5[t]);
    } else {
        const int tt = u - (PREP_WO + PREP_W2), tok = lane & 15;
        u32x4 v[5]; float sum = 0.f;
#pragma unroll
        for (int i = 0; i < 5; ++i) v[i] = *(const u32x4*)(a.x + (size_t)(16 * tt + tok) * DM + 32 * (wave * 5 + i) + 8 * q);
#pragma unroll
        for (int i = 0; i < 5; ++i) { prep_st(a, WS_X + (tt * 20 + wave * 5 + i) * KB + lane * 16, clean4(v[i])); sum += frag_sum(v[i]); }
        sum = sum_q(sum);
        red[wave][lane] = sum; __syncthreads();
        const float mean = ((red[0][tok] + red[1][tok]) + (red[2][tok] + red[3][tok])) * (1.f / DM);
        __syncthreads();
        float m2 = 0.f;
#pragma unroll
        for (int i = 0; i < 5; ++i)
#pragma unroll
            for (int e = 0; e < 4; ++e) { const float d0 = bf_lo(v[i][e]) - mean, d1 = bf_hi(v[i][e]) - mean; m2 += d0 * d0 + d1 * d1; }
        m2 = sum_q(m2);
        red[wave][lane] = m2; __syncthreads();
        if (tid < 128) {
            const int c8 = tid >> 4, tk = tid & 15;
            const float mean_t = __shfl(mean, tk);
            const float tot = (red[0][tk] + red[1][tk]) + (red[2][tk] + red[3][tk]);
            const int g = tt >> 3, tl = (tt & 7) * 16 + tk;
            *(u32x2*)(a.ws + WS_ST + ((g * 8 + c8) * 128 + tl) * 8) = (u32x2){clean(__float_as_uint(mean_t)), clean(__float_as_uint(c8 == 0 ? tot : 0.f))};
        }
    }
}

struct MainArgs { char* ws; u16* out; };

struct Ctx {
    __amdgpu_buffer_rsrc_t rs;
    char* ws; u16* out;
    lds_ptr lds; unsigned lds0;
    int g, j, wave, lane;
};

DI Ctx fresh(const Ctx& c0) {
    Ctx c = c0;
    asm volatile("" : "+v"(c.lane)); asm volatile("" : "+s"(c.g)); asm volatile("" : "+s"(c.j)); asm volatile("" : "+s"(c.wave)); asm volatile("" : "+s"(c.lds0));
    return c;
}
DI u32x4 ld128(const Ctx& c, unsigned voff, unsigned soff) { return __builtin_amdgcn_raw_buffer_load_b128(c.rs, voff, soff, AUX_LD); }
DI unsigned live_off(bool live, unsigned off) { asm volatile("" : "+v"(off)); return live ? off : OOB; }
DI u32x2 ld64(const Ctx& c, unsigned voff, unsigned soff) { return __builtin_amdgcn_raw_buffer_load_b64(c.rs, voff, soff, AUX_SC1); }
DI float ld32_plain(const Ctx& c, unsigned voff, unsigned soff) { return __uint_as_float(__builtin_amdgcn_raw_buffer_load_b32(c.rs, voff, soff, 0)); }
DI void st128(const Ctx& c, u32x4 v, unsigned voff, unsigned soff) {
    __builtin_amdgcn_raw_buffer_store_b128(v, c.rs, voff, soff, AUX_SC1);
    asm volatile("s_nop 1" :: "v"(v) : "memory");
}
DI void st64(const Ctx& c, u32x2 v, unsigned voff, unsigned soff) { __builtin_amdgcn_raw_buffer_store_b64(v, c.rs, voff, soff, AUX_SC1); }

#define STAMP(c, L, i) do { } while (0)
#define TICK(v) do { } while (0)
#define TICKSTORE(c, L, i, v) do { } while (0)
DI unsigned flag_off(int L, int ph, int g) { return WS_FLAGS + (L * 16 + g) * 4096 + (ph == 0 ? 0 : 2048 + (ph - 1) * 512); }
DI void st32(const Ctx& c, unsigned v, unsigned voff, unsigned soff) { __builtin_amdgcn_raw_buffer_store_b32(v, c.rs, voff, soff, AUX_SC1); }
DI void signal(const Ctx& c, int L, int ph, int idx) { st32(c, 1u, c.lane == 0 ? 0u : OOB, flag_off(L, ph, c.g) + idx * 4); }
constexpr unsigned SPIN_CAP = 1u << 18;
DI void timed_out(const Ctx& c) { st32(c, 1u, c.lane == 0 ? 0u : OOB, WS_TMO); }
DI void await(const Ctx& c, unsigned off, bool use) {
    unsigned spins = 0;
    const unsigned vo = live_off(use, off);
    if (AWAIT_DELAY) __builtin_amdgcn_s_sleep(AWAIT_DELAY);
    for (;;) {
        const unsigned v = __builtin_amdgcn_raw_buffer_load_b32(c.rs, vo, 0, AUX_SC1);
        if (__all(v == 1u || !use)) break;
        if (++spins > SPIN_CAP) { timed_out(c); break; }
        if (POLL_SLEEP) __builtin_amdgcn_s_sleep(POLL_SLEEP);
        asm volatile("" ::: "memory");
    }
}
DI void canary(const Ctx& c, unsigned voff, unsigned soff) {
    if (CANARY_DELAY) __builtin_amdgcn_s_sleep(CANARY_DELAY);
    unsigned spins = 0;
    for (;;) {
        const unsigned v = __builtin_amdgcn_raw_buffer_load_b32(c.rs, voff, soff, AUX_SC1);
        if (!missing(v)) break;
        if (++spins > SPIN_CAP) { timed_out(c); break; }
        asm volatile("" ::: "memory");
    }
}
#define RF_POLL(i) \
    "s_mov_b32 %[n], 0\n" \
    "1" #i ":\n\t" \
    "buffer_load_dword %[t0], %[vo0], %[rs], %[so] offen offset:" #i "*1024 sc1\n\t" \
    "buffer_load_dword %[t1], %[vo0], %[rs], %[so] offen offset:" #i "*1024+8 sc1\n\t" \
    "buffer_load_dword %[t2], %[vo1], %[rs], %[so] offen offset:" #i "*1024 sc1\n\t" \
    "buffer_load_dword %[t3], %[vo1], %[rs], %[so] offen offset:" #i "*1024+8 sc1\n\t" \
    "s_add_u32 %[n], %[n], 1\n\t" \
    "s_waitcnt vmcnt(0)\n\t" \
    "v_max3_u32 %[t0], %[t0], %[t1], %[t2]\n\t" \
    "v_max_u32 %[t0], %[t0], %[t3]\n\t" \
    "v_cmp_eq_u32 vcc, -1, %[t0]\n\t" \
    "s_cmp_gt_u32 %[n], %[cap]\n\t" \
    "s_cbranch_scc1 2" #i "f\n\t" \
    "s_nop 4\n\t" \
    "s_cbranch_vccnz 1" #i "b\n" \
    "2" #i ":\n\t"
#define RF_LOAD(i, a, b) \
    "buffer_load_dwordx4 %[" #a "], %[vo0], %[rs], %[so] offen offset:" #i "*1024 sc1\n\t" \
    "buffer_load_dwordx4 %[" #b "], %[vo1], %[rs], %[so] offen offset:" #i "*1024 sc1\n\t"
#define RF_HEAD "s_cmp_lg_u64 %[late], 0\n\ts_cbranch_scc1 8f\n9:\n\t.subsection 1\n8:\n\ts_nop 4\n\t"
#define RF_TAIL "s_waitcnt vmcnt(0)\n\ts_branch 9b\n\t.subsection 0"
#define RF_TMP [t0] "=&v"(t0), [t1] "=&v"(t1), [t2] "=&v"(t2), [t3] "=&v"(t3), [n] "=&s"(n)
#define RF_IN [late] "s"(late), [vo0] "v"(vo0), [vo1] "v"(vo1), [rs] "s"(c.rs), [so] "s"(so), [cap] "s"(SPIN_CAP)
typedef unsigned long long ballot_t;
DI ballot_t late_lanes(unsigned m) { return __ballot(m == SENT); }
DI void refetch(const Ctx& c, ballot_t late, u32x4 (&p0)[2], unsigned vo0, unsigned vo1, unsigned so) {
    unsigned t0, t1, t2, t3, n;
    asm volatile(RF_HEAD RF_POLL(0) RF_LOAD(0, a0, b0) RF_TAIL
                 : [a0] "+v"(p0[0]), [b0] "+v"(p0[1]), RF_TMP : RF_IN : "vcc", "scc", "memory");
}
DI void refetch(const Ctx& c, ballot_t late, u32x4 (&p0)[2], u32x4 (&p1)[2], unsigned vo0, unsigned vo1, unsigned so) {
    unsigned t0, t1, t2, t3, n;
    asm volatile(RF_HEAD RF_POLL(0) RF_POLL(1) RF_LOAD(0, a0, b0) RF_LOAD(1, a1, b1) RF_TAIL
                 : [a0] "+v"(p0[0]), [b0] "+v"(p0[1]), [a1] "+v"(p1[0]), [b1] "+v"(p1[1]), RF_TMP : RF_IN : "vcc", "scc", "memory");
}
DI void refetch(const Ctx& c, ballot_t late, u32x4 (&p0)[2], u32x4 (&p1)[2], u32x4 (&p2)[2], unsigned vo0, unsigned vo1, unsigned so) {
    unsigned t0, t1, t2, t3, n;
    asm volatile(RF_HEAD RF_POLL(0) RF_POLL(1) RF_POLL(2) RF_LOAD(0, a0, b0) RF_LOAD(1, a1, b1) RF_LOAD(2, a2, b2) RF_TAIL
                 : [a0] "+v"(p0[0]), [b0] "+v"(p0[1]), [a1] "+v"(p1[0]), [b1] "+v"(p1[1]), [a2] "+v"(p2[0]), [b2] "+v"(p2[1]), RF_TMP : RF_IN : "vcc", "scc", "memory");
}
DI void refetch(const Ctx& c, ballot_t late, u32x4 (&p0)[2], u32x4 (&p1)[2], u32x4 (&p2)[2], u32x4 (&p3)[2], unsigned vo0, unsigned vo1, unsigned so) {
    unsigned t0, t1, t2, t3, n;
    asm volatile(RF_HEAD RF_POLL(0) RF_POLL(1) RF_POLL(2) RF_POLL(3) RF_LOAD(0, a0, b0) RF_LOAD(1, a1, b1) RF_LOAD(2, a2, b2) RF_LOAD(3, a3, b3) RF_TAIL
                 : [a0] "+v"(p0[0]), [b0] "+v"(p0[1]), [a1] "+v"(p1[0]), [b1] "+v"(p1[1]), [a2] "+v"(p2[0]), [b2] "+v"(p2[1]), [a3] "+v"(p3[0]), [b3] "+v"(p3[1]), RF_TMP
                 : RF_IN : "vcc", "scc", "memory");
}
#define RF1_POLL(i) \
    "s_mov_b32 %[n], 0\n" \
    "1" #i ":\n\t" \
    "buffer_load_dword %[t0], %[vo], %[rs], %[s" #i "] offen sc1\n\t" \
    "buffer_load_dword %[t1], %[vo], %[rs], %[s" #i "] offen offset:8 sc1\n\t" \
    "s_add_u32 %[n], %[n], 1\n\t" \
    "s_waitcnt vmcnt(0)\n\t" \
    "v_max_u32 %[t0], %[t0], %[t1]\n\t" \
    "v_cmp_eq_u32 vcc, -1, %[t0]\n\t" \
    "s_cmp_gt_u32 %[n], %[cap]\n\t" \
    "s_cbranch_scc1 2" #i "f\n\t" \
    "s_nop 4\n\t" \
    "s_cbranch_vccnz 1" #i "b\n" \
    "2" #i ":\n\t"
#define RF1_LOAD(i) "buffer_load_dwordx4 %[a" #i "], %[vo], %[rs], %[s" #i "] offen sc1\n\t"
#define RF1_TMP [t0] "=&v"(t0), [t1] "=&v"(t1), [n] "=&s"(n)
#define RF1_IN [late] "s"(late), [vo] "v"(vo), [rs] "s"(c.rs), [cap] "s"(SPIN_CAP)
DI void refetch1(const Ctx& c, ballot_t late, u32x4& a0, u32x4& a1, unsigned vo, unsigned s0, unsigned s1) {
    unsigned t0, t1, n;
    asm volatile(RF_HEAD RF1_POLL(0) RF1_POLL(1) RF1_LOAD(0) RF1_LOAD(1) RF_TAIL
                 : [a0] "+v"(a0), [a1] "+v"(a1), RF1_TMP : RF1_IN, [s0] "s"(s0), [s1] "s"(s1) : "vcc", "scc", "memory");
}
DI void refetch1(const Ctx& c, ballot_t late, u32x4& a0, u32x4& a1, u32x4& a2, u32x4& a3, unsigned vo, unsigned s0, unsigned s1, unsigned s2, unsigned s3) {
    unsigned t0, t1, n;
    asm volatile(RF_HEAD RF1_POLL(0) RF1_POLL(1) RF1_POLL(2) RF1_POLL(3) RF1_LOAD(0) RF1_LOAD(1) RF1_LOAD(2) RF1_LOAD(3) RF_TAIL
                 : [a0] "+v"(a0), [a1] "+v"(a1), [a2] "+v"(a2), [a3] "+v"(a3), RF1_TMP
                 : RF1_IN, [s0] "s"(s0), [s1] "s"(s1), [s2] "s"(s2), [s3] "s"(s3) : "vcc", "scc", "memory");
}
DI void st_fill(const Ctx& c, unsigned voff, unsigned soff) {
    unsigned m; asm volatile("v_mov_b32 %0, -1" : "=v"(m));
    st128(c, (u32x4){m, m, m, m}, voff, soff);
}
template <int K> DI void fill_x(const Ctx& c, int xi, int i) {
    const int th = c.j >> 3, c8 = c.j & 7, sp = (5 * c8 + 1) >> 1, gt = (c8 & 1) ? 5 * c8 : 5 * c8 + 4;
    const unsigned tb = WS_X + xi * XB + (c.g * 8 + 4 * th + i) * 20 * KB, all = c.lane * 16;
    if (K < 2) st_fill(c, all, tb + (sp + K) * KB);
    if (K == 2) st_fill(c, live_off(c.lane < 32, all), tb + gt * 512);
    if (K == 3) st_fill(c, live_off(c.lane < 32 && i == 0, all), WS_ST + xi * STB + ((c.g * 8 + c8) * 128 + 64 * th) * 8);
}
template <int K> DI void fill_h(const Ctx& c, int L, int i) {
    const int sp = (7 * c.j + 1) >> 1, gt = (c.j & 1) ? 7 * c.j : 7 * c.j + 6;
    const unsigned tb = WS_H + L * HB + (c.g * 8 + i) * 54 * KB, all = c.lane * 16;
    if (K < 3) st_fill(c, live_off(sp + K < 54, all), tb + (sp + K < 54 ? sp + K : 0) * KB);
    else st_fill(c, live_off(c.lane < 32, all), tb + gt * 512);
}
template <int K> DI void fill_o(const Ctx& c, int L, int i) {
    st_fill(c, K == 0 ? c.lane * 16 : live_off(c.lane < 16, c.lane * 16), WS_O + L * XB + (c.g * 8 + i) * 20 * KB + 5 * c.j * 256 + K * KB);
}

struct Loader { unsigned voff; unsigned src[4]; int lw; };
template <int M> DI void fill_chunk(const Ctx& c, const Loader& l) {
    constexpr int n = M % NCH, size = ch_size(n), q = size / 4, r = size % 4;
    static_assert(r == 0 || r == 2, "chunk sizes");
    const int lw = l.lw;
    const unsigned start = (unsigned)(r == 0 ? lw * q : (n & 1) ? max(q * lw, (q + 1) * lw - 2) : min((q + 1) * lw, q * lw + 2));
    const unsigned dst = c.lds0 + SC.pos[n] * KB + start * KB, src = l.src[ch_phase(n)] + ch_src(n) * KB + start * KB;
#define DMA1(o) "buffer_load_dwordx4 %1, %2, %3 offen offset:" #o " lds\n\t"
    if constexpr (q >= 4) asm volatile("s_mov_b32 m0, %0\n\ts_nop 0\n\t" DMA1(0) DMA1(1024) DMA1(2048) DMA1(3072) :: "s"(dst), "v"(l.voff), "s"(c.rs), "s"(src) : "memory");
    if constexpr (q == 3) asm volatile("s_mov_b32 m0, %0\n\ts_nop 0\n\t" DMA1(0) DMA1(1024) DMA1(2048) :: "s"(dst), "v"(l.voff), "s"(c.rs), "s"(src) : "memory");
    if constexpr (q == 2) asm volatile("s_mov_b32 m0, %0\n\ts_nop 0\n\t" DMA1(0) DMA1(1024) :: "s"(dst), "v"(l.voff), "s"(c.rs), "s"(src) : "memory");
    if constexpr (q == 5) asm volatile("s_mov_b32 m0, %0\n\ts_nop 0\n\t" DMA1(0) :: "s"(dst + 4 * KB), "v"(l.voff), "s"(c.rs), "s"(src + 4 * KB) : "memory");
    if constexpr (r != 0) {
        if ((n & 1) ? lw >= 2 : lw < 2) {
            if constexpr (q == 3) asm volatile(DMA1(3072) :: "s"(dst), "v"(l.voff), "s"(c.rs), "s"(src) : "memory");
            else asm volatile(DMA1(2048) :: "s"(dst), "v"(l.voff), "s"(c.rs), "s"(src) : "memory");
        }
    }
#undef DMA1
}
template <int B, int E, bool EXT = false> DI void fill_range(const Ctx& c, const Loader& l, bool more = true) {
    if constexpr (B < E) {
        if constexpr (ch_ext(B % NCH) == EXT) { if (B >= NCH ? more : (B >= PRO || !more)) fill_chunk<B>(c, l); }
        fill_range<B + 1, E, EXT>(c, l, more);
    }
}
template <int EV> DI void loader_event(const Ctx& c0, const Loader& l0, int L) {
    if constexpr (SC.need[EV] >= 0) {
        if (L == 0) asm volatile("s_waitcnt vmcnt(%0) lgkmcnt(0)\n\ts_barrier" :: "i"(SC.wait_first[EV]) : "memory");
        else asm volatile("s_waitcnt vmcnt(%0) lgkmcnt(0)\n\ts_barrier" :: "i"(SC.wait_last[EV]) : "memory");
    }
    if constexpr (SC.need[EV] < 0) asm volatile("s_waitcnt lgkmcnt(0)\n\ts_barrier" ::: "memory");
    if constexpr (EV >= EV_B1 && EV < EV_B1 + 5) {
        constexpr int J = EV - EV_B1;
        static_assert(SC.fb[EV_B1 + 4] <= C3, "order of the first sentinels");
        if (L == 0) {
            const Ctx c = fresh(c0); const int lw = c.wave - 4;
            const unsigned kb = WS_KV + (256 + c.g * 16 + c.j) * KVB + lw * KB;
            if constexpr (J < 3) {
                st_fill(c, c.lane * 16, kb + 2 * J * 4 * KB);
                if constexpr (J < 2) st_fill(c, c.lane * 16, kb + (2 * J + 1) * 4 * KB); else st_fill(c, live_off(lw < 2, c.lane * 16), kb + 20 * KB);
            } else { fill_x<2 * (J - 3)>(c, 1, lw); fill_x<2 * (J - 3) + 1>(c, 1, lw); }
        }
    }
    if constexpr (EV >= EV_B4 && EV < EV_B4 + 20) {
        constexpr int J = EV - EV_B4;
        static_assert(J >= 4 || (SC.fb[EV_B4 + 4] < SC.fe[EV_B4 + 4] && SC.fb[EV_B4 + 4] < C5), "a chunk of this loop is requested behind the X2 sentinels");
        if (L == 0) {
            const Ctx c = fresh(c0); const int lw = c.wave - 4;
            if constexpr (J < 4) fill_x<J>(c, 2, lw);
            else if constexpr (J < 8) fill_x<J - 4>(c, 3, lw);
            else if constexpr (J < 12) fill_o<J & 1>(c, 1, lw + 4 * ((J - 8) >> 1));
            else if constexpr (J < 16) fill_h<J - 12>(c, 1, lw);
            else fill_h<J - 16>(c, 1, lw + 4);
        }
    }
    if constexpr (SC.fb[EV] < SC.fe[EV]) {
        Ctx c = c0; Loader l = l0;
        asm volatile("" : "+s"(c.lds0)); asm volatile("" : "+s"(l.lw));
        asm volatile("" : "+s"(l.src[0])); asm volatile("" : "+s"(l.src[1])); asm volatile("" : "+s"(l.src[2])); asm volatile("" : "+s"(l.src[3]));
        fill_range<SC.fb[EV], SC.fe[EV]>(c, l, L == 0);
    }
}
constexpr int EV_E3 = EV_E1 + 2;
static_assert(SC.fb[EV_E1 + 1] == SC.fe[EV_E1 + 1] && SC.fb[EV_E1 + 3] == SC.fe[EV_E1 + 3], "ring fills inside the attention phase only at E3");
template <int B, int E> DI void loader_events(const Ctx& c, const Loader& l, int L) { if constexpr (B < E) { loader_event<B>(c, l, L); loader_events<B + 1, E>(c, l, L); } }

struct Stats { u32x2 p[2][2]; };
DI void stats_load(const Ctx& c, unsigned stin, Stats& st) {
    const int w = c.wave, tok = c.lane & 15, q = c.lane >> 4;
#pragma unroll
    for (int m = 0; m < 2; ++m)
#pragma unroll
        for (int i = 0; i < 2; ++i) st.p[m][i] = ld64(c, ((2 * q + i) * 128 + 32 * w + 16 * m + tok) * 8, stin);
}
DI void stats_wait(const Ctx& c, unsigned stin, bool delay, Stats& st) {
    const int w = c.wave, tok = c.lane & 15, q = c.lane >> 4;
    if (delay && CANARY_DELAY) __builtin_amdgcn_s_sleep(CANARY_DELAY);
    unsigned spins = 0;
    for (;;) {
#pragma unroll
        for (int m = 0; m < 2; ++m)
#pragma unroll
            for (int i = 0; i < 2; ++i) st.p[m][i] = ld64(c, ((2 * q + i) * 128 + 32 * w + 16 * m + tok) * 8, stin);
        if (!missing(umax3(umax3(umax3(st.p[0][0][0], st.p[0][0][1], st.p[0][1][0]), st.p[0][1][1], st.p[1][0][0]), umax3(st.p[1][0][1], st.p[1][1][0], st.p[1][1][1]), 0u))) break;
        if (++spins > SPIN_CAP) { timed_out(c); break; }
        asm volatile("" ::: "memory");
    }
}
DI void stats_reduce(const Stats& st, float (&mean)[2], float (&var)[2]) {
#pragma unroll
    for (int m = 0; m < 2; ++m) {
        const float m0 = __uint_as_float(st.p[m][0][0]), m1 = __uint_as_float(st.p[m][1][0]);
        mean[m] = sum_q(m0 + m1) * 0.125f;
        const float d0 = m0 - mean[m], d1 = m1 - mean[m];
        var[m] = sum_q(__uint_as_float(st.p[m][0][1]) + __uint_as_float(st.p[m][1][1]) + 80.f * (d0 * d0 + d1 * d1)) * (1.f / DM) + LN_EPS;
    }
}

DI void p1_compute(const Ctx& c0, int L) {
    const Ctx c = fresh(c0);
    constexpr int NB = NB1;
    const int w = c.wave, lane = c.lane, tok = lane & 15, q = lane >> 4;
    const unsigned xin = WS_X + (2 * L) * XB + (c.g * 8 + 2 * w) * 20 * KB, stin = WS_ST + (2 * L) * STB + c.g * 8 * 128 * 8;
    STAMP(c, L, 0);
    Stats st;
    if (L > 0) stats_wait(c, stin, true, st); else stats_load(c, stin, st);
    STAMP(c, L, 1);
#define LDX(s, m) ld128(c, lane * 16 + (m) * 20 * KB, xin + (s) * KB)
    u32x4 bf[NB][2];
#pragma unroll
    for (int s = 0; s < 2; ++s)
#pragma unroll
        for (int m = 0; m < 2; ++m) bf[s][m] = LDX(s, m);
    BAR_LGKM();
    STAMP(c, L, 2);
#pragma unroll
    for (int s = 2; s < WIN0; ++s)
#pragma unroll
        for (int m = 0; m < 2; ++m) bf[s][m] = LDX(s, m);
    float cs_q = 0.f, cs_h = 0.f;
    const Ring ring = make_ring((lds_cptr)c.lds + lane * 16);
    bf16x8 a[2][8];
#pragma unroll
    for (int t = 0; t < 8; ++t) a[0][t] = ring_frag(ring, (SC.pos[C1] + t) * KB);
    float mean[2], rstd[2];
    stats_reduce(st, mean, rstd);
#pragma unroll
    for (int m = 0; m < 2; ++m) { rstd[m] = __builtin_amdgcn_rsqf(rstd[m]); asm volatile("" : "+v"(mean[m]), "+v"(rstd[m])); }
    f32x4 cs[8];
    f32x4 acc[8][2];
    constexpr int G = 2;
    unsigned mx = 0;
#pragma unroll
    for (int i = 0; i < G; ++i) mx = chk(bf[i][0], chk(bf[i][1], mx));
    ballot_t late = late_lanes(mx);
    static_for<0, 20>([&](auto s_) __attribute__((always_inline)) {
        constexpr int s = decltype(s_)::value;
        if (s % G == 0) {
            refetch(c, late, bf[s % NB], bf[(s + 1) % NB], lane * 16, lane * 16 + 20 * KB, xin + s * KB);
            mx = 0;
        }
        if (s == 12 && L == 0) {
            const int t = w * 64 + lane;
            *(LAS float*)(c.lds + (t < 128 ? L_CSQ + t * 4 : L_END)) = cs_q;
            *(LAS float*)(c.lds + (t < 224 ? L_CS13 + t * 4 : L_END)) = cs_h;
        }
        if ((s & 1) && s + 1 < 20) BAR_LGKM();
        SBAR();
#pragma unroll
        for (int t = 0; t < 8; ++t) {
            MF16(s == 0, acc[t][0], a[s & 1][t], as_frag(bf[s % NB][0]));
            if (s + 1 < 20 && t < 4) a[(s + 1) & 1][7 - 2 * t] = ring_frag(ring, (SC.pos[C1 + (s + 1) / 2] + ((s + 1) & 1) * 8 + 7 - 2 * t) * KB);
            if (s == 19) cs[t] = *(const LAS f32x4*)((lds_cptr)c.lds + L_CSQ + (t * 16 + 4 * q) * 4);
            SBAR();
            MF16(s == 0, acc[t][1], a[s & 1][t], as_frag(bf[s % NB][1]));
            if (s + 1 < 20 && t < 4) a[(s + 1) & 1][6 - 2 * t] = ring_frag(ring, (SC.pos[C1 + (s + 1) / 2] + ((s + 1) & 1) * 8 + 6 - 2 * t) * KB);
            if ((s / G + 1) * G + s % G < 20 && t >= 6) mx = chk(bf[((s / G + 1) * G + s % G) % NB][t & 1], mx);
            SBAR();
        }
#pragma unroll
        for (int k = (s == 0 ? WIN0 : win_upto(s - 1, NB, 20, 2)); k < win_upto(s, NB, 20, 2); ++k)
#pragma unroll
            for (int m = 0; m < 2; ++m) bf[k % NB][m] = LDX(k, m);
        if (s == 4 && L == 0) {
            const int t = w * 64 + lane;
            cs_q = ld32_plain(c, live_off(t < 128, t * 4), WS_CSQ + c.j * 512);
            cs_h = ld32_plain(c, live_off(t < 224, t * 4), WS_CS13 + c.j * 896);
        }
        if (s % G == G - 1 && s + 1 < 20) late = late_lanes(mx);
        SBAR();
    });
    unsigned long long tq[6] = {};
    TICK(tq[0]);
    MFMA_DONE();
#pragma unroll
    for (int t = 0; t < 8; ++t) { PIN4(acc[t][0]); PIN4(acc[t][1]); }
#undef LDX
    TICK(tq[1]);
    STAMP(c, L, 3);
    TICK(tq[2]);
    const unsigned kv = WS_KV + (L * 256 + c.g * 16 + c.j) * KVB;
    const int hd = q >> 1, hf = q & 1;
    u32x4 pk_[2], pv_[2]; u32x2 t6_[2], t7_[2];
    const f32x2 nm2[2] = {splat2(-mean[0]), splat2(-mean[1])}, r2[2] = {splat2(rstd[0]), splat2(rstd[1])};
#define QKV_PK(t, m, hh) ({ const f32x2 v_ = fma2(hh ? hi2(cs[t]) : lo2(cs[t]), nm2[m], hh ? hi2(acc[t][m]) : lo2(acc[t][m])) * r2[m]; pk_bf16(v_[0], v_[1]); })
#pragma unroll
    for (int m = 0; m < 2; ++m) {
        const int key = 16 * m + tok;
        pk_[m] = clean4((u32x4){QKV_PK(2, m, 0), QKV_PK(2, m, 1), QKV_PK(3, m, 0), QKV_PK(3, m, 1)});
        pv_[m] = clean4((u32x4){QKV_PK(4, m, 0), QKV_PK(4, m, 1), QKV_PK(5, m, 0), QKV_PK(5, m, 1)});
        t6_[m] = clean2((u32x2){QKV_PK(6, m, 0), QKV_PK(6, m, 1)});
        t7_[m] = clean2((u32x2){QKV_PK(7, m, 0), QKV_PK(7, m, 1)});
        if (q == 2) t7_[m] = (u32x2){0x00003f80u, 0u};
        if (q == 3) t7_[m] = (u32x2){0u, 0u};
        st128(c, pk_[m], hd * 4096 + w * 1024 + hf * 512 + key * 16, kv);
        st128(c, pv_[m], 8192 + hd * 4096 + (w * 32 + key) * 32 + hf * 16, kv);
        st64(c, t6_[m], live_off(q >= 2, 20480 + w * 512 + (q - 2) * 256 + key * 8), kv);
        st64(c, t7_[m], 16384 + (w * 32 + key) * 32 + q * 8, kv);
        if (m == 0) TICK(tq[3]); else TICK(tq[4]);
    }
#pragma unroll
    for (int m = 0; m < 2; ++m) {
        const int key = 16 * m + tok;
        *(LAS u32x4*)(c.lds + L_QM + hd * 4096 + w * 1024 + hf * 512 + key * 16) = (u32x4){QKV_PK(0, m, 0), QKV_PK(0, m, 1), QKV_PK(1, m, 0), QKV_PK(1, m, 1)};
        *(LAS u32x4*)(c.lds + L_KA + hd * 12288 + (8 + w) * 1024 + hf * 512 + key * 16) = pk_[m];
        *(LAS u32x4*)(c.lds + L_VA + hd * 12288 + ((8 + w) * 32 + key) * 32 + hf * 16) = pv_[m];
        *(LAS u32x2*)(c.lds + (q < 2 ? L_QT + w * 512 + q * 256 : L_KT + (8 + w) * 512 + (q - 2) * 256) + key * 8) = t6_[m];
        *(LAS u32x2*)(c.lds + L_VT + ((8 + w) * 32 + key) * 32 + q * 8) = t7_[m];
    }
#undef QKV_PK
#pragma unroll
    for (int i = 0; i < 5; ++i) TICKSTORE(c, L, 8 + i, tq[i]);
    STAMP(c, L, 4);
    if (L == 0) { BAR_ALL(); signal(c, L, 0, c.j * 32 + w); }
    else BAR_LGKM();
    STAMP(c, L, 5);
}

template <bool PARTIAL> DI f32x16 attn_body(const Ctx& c, const bf16x8 qm, const s16x4 qt, const f32x16 b_old, const f32x16 b_diag, int lt0) {
    const int lane = c.lane, hh = c.wave >> 2, w = c.wave & 3, h = lane >> 5;
    const lds_cptr lb = (lds_cptr)c.lds;
    const lds_cptr kmb = lb + L_KA + hh * 12288 + w * 1024 + lane * 16, ktb = lb + L_KT + w * 512 + lane * 8;
    const lds_cptr vb = lb + (((lane >> 4) & 1) ? L_VT : L_VA + hh * 12288) + w * 1024 + (4 * h + ((lane & 15) >> 2)) * 32 + (lane & 3) * 8;
    const float NEG = -__builtin_inff();
    f32x16 s[9];
    bf16x8 km[3]; u32x2 k2[3];
    float mx[4] = {NEG, NEG, NEG, NEG};
#pragma unroll
    for (int kt = 0; kt < 2; ++kt) { km[kt] = lds_frag(kmb + kt * 1024); k2[kt] = *(const LAS u32x2*)(ktb + kt * 512); }
#pragma unroll
    for (int kt = 0; kt < 9; ++kt) {
        if (kt + 2 < 9) { km[(kt + 2) % 3] = lds_frag(kmb + (kt + 2) * 1024); k2[(kt + 2) % 3] = *(const LAS u32x2*)(ktb + (kt + 2) * 512); }
        f32x16 z;
        if (kt == 0) z = b_old; else if (kt == 8) z = b_diag;
        else {
#pragma unroll
            for (int r = 0; r < 16; ++r) z[r] = 0.f;
        }
        if (PARTIAL && kt < 8) {
            const bool dead = w + kt < lt0;
#pragma unroll
            for (int r = 0; r < 16; ++r) z[r] = dead ? NEG : z[r];
        }
        z = MFMA32(km[kt % 3], qm, z);
        s[kt] = __builtin_amdgcn_mfma_f32_32x32x8bf16_1k(__builtin_bit_cast(s16x4, k2[kt % 3]), qt, z, 0, 0, 0);
        if (kt >= 2) {
#pragma unroll
            for (int r = 0; r < 16; r += 2) mx[(r >> 1) & 3] = fmaxf(fmaxf(mx[(r >> 1) & 3], s[kt - 2][r]), s[kt - 2][r + 1]);
        }
        if (!PARTIAL) {
            if (kt + 2 < 9) SGB(SG_DSR, 2);
            SGB(SG_MFMA, 1); if (kt >= 2) SGB(SG_VALU, 4); SGB(SG_MFMA, 1); if (kt >= 2) SGB(SG_VALU, 4);
        }
        SBAR();
    }
#pragma unroll
    for (int kt = 7; kt < 9; ++kt)
#pragma unroll
        for (int r = 0; r < 16; r += 2) mx[(r >> 1) & 3] = fmaxf(fmaxf(mx[(r >> 1) & 3], s[kt][r]), s[kt][r + 1]);
    const float m = swap32_max(fmaxf(fmaxf(mx[0], mx[1]), fmaxf(mx[2], mx[3])));
    constexpr float CS = 0.22360679774997896f * 1.4426950408889634f;
    const f32x2 cs2 = {CS, CS}, nm2 = {-m * CS, -m * CS};
    f32x16 o = {};
    s16x4 vf[2][2];
    f32x2 tt[2]; float e0[2], e1[2]; unsigned pw[2][4];
    vf[0][0] = tr_read(vb); vf[0][1] = tr_read(vb + 256);
    SBAR();
    static_for<0, 75>([&](auto i_) __attribute__((always_inline)) {
        constexpr int i = decltype(i_)::value;
        if constexpr (i < 72) tt[i & 1] = fma2((f32x2){s[i / 8][2 * (i % 8)], s[i / 8][2 * (i % 8) + 1]}, cs2, nm2);
        if constexpr (i >= 1 && i <= 72) { e0[(i - 1) & 1] = __builtin_amdgcn_exp2f(tt[(i - 1) & 1][0]); e1[(i - 1) & 1] = __builtin_amdgcn_exp2f(tt[(i - 1) & 1][1]); }
        if constexpr (i >= 2 && i <= 73) pw[((i - 2) >> 2) & 1][(i - 2) & 3] = pk_bf16(e0[i & 1], e1[i & 1]);
        if constexpr (i >= 3 && (i - 3) % 4 == 0 && (i - 3) / 4 + 1 < 18) {
            constexpr int gn = (i - 3) / 4 + 1;
            vf[gn & 1][0] = tr_read(vb + gn * 512); vf[gn & 1][1] = tr_read(vb + gn * 512 + 256);
        }
        if constexpr (i >= 6 && (i - 6) % 4 == 0) {
            constexpr int gq = (i - 6) / 4;
            o = MFMA32(__builtin_shufflevector(vf[gq & 1][0], vf[gq & 1][1], 0, 1, 2, 3, 4, 5, 6, 7),
                       as_frag((u32x4){pw[gq & 1][0], pw[gq & 1][1], pw[gq & 1][2], pw[gq & 1][3]}), o);
        }
        SBAR();
    });
    return o;
}

DI void p2_attention(const Ctx& c0, const Loader& l0, int L) {
    const Ctx c = fresh(c0);
    const int lane = c.lane, hh = c.wave >> 2, w = c.wave & 3, g = c.g;
    {
        const int gp = g - 1 - ((lane >> 2) & 1);
        if (L == 0) await(c, flag_off(L, 0, gp < 0 ? 0 : gp) + (c.j * 32 + (lane & 3)) * 4, lane < 8 && gp >= 0);
        else if (KV_DELAY < 0) canary(c, live_off(gp >= 0, (gp < 0 ? 0 : gp) * 16 * KVB + 16384 + ((lane & 3) * 32 + 16) * 32), WS_KV + (256 + c.j) * KVB);
        else __builtin_amdgcn_s_sleep(KV_DELAY < 0 ? 0 : KV_DELAY);
    }
    STAMP(c, L, 6);
    unsigned long long ta[8] = {};
    TICK(ta[0]);
    u32x4 v[6];
    const int gi = c.wave >> 2, u = c.wave & 3, gs = g - 2 + gi;
    const unsigned so = WS_KV + (L * 256 + (gs < 0 ? 0 : gs) * 16 + c.j) * KVB + u * KB, vo = live_off(gs >= 0, lane * 16), vt = live_off(gs >= 0 && u < 2, lane * 16);
#pragma unroll
    for (int i = 0; i < 5; ++i) v[i] = ld128(c, vo, so + i * 4 * KB);
    v[5] = ld128(c, vt, so + 20 * KB);
    const int h = lane >> 5, qi = lane & 31;
    const lds_cptr lb = (lds_cptr)c.lds;
    const bf16x8 qm = lds_frag(lb + L_QM + hh * 4096 + w * 1024 + lane * 16);
    u32x2 qt2 = *(const LAS u32x2*)(lb + L_QT + w * 512 + lane * 8);
    if (h != hh) qt2 = (u32x2){0u, 0u};
    const s16x4 qt = __builtin_bit_cast(s16x4, qt2);
    f32x16 b_old, b_diag;
    {
        const float NEG = -__builtin_inff();
        const int d = qi - 4 * h;
#pragma unroll
        for (int r = 0; r < 16; ++r) {
            const bool newer = (r & 3) + 8 * (r >> 2) > d;
            b_old[r] = newer ? 0.f : NEG;
            b_diag[r] = newer ? NEG : 0.f;
        }
        asm volatile("" : "+v"(b_old), "+v"(b_diag));
    }
    TICK(ta[1]);
    {
        unsigned sp_ = 0;
        while (__builtin_expect(missing(umax4(v[0], umax4(v[1], umax4(v[2], umax4(v[3], umax4(v[4], umax4(v[5], 0u))))))), 0) && ++sp_ < SPIN_CAP) {
#pragma unroll
            for (int i = 0; i < 5; ++i) v[i] = ld128(c, vo, so + i * 4 * KB);
            v[5] = ld128(c, vt, so + 20 * KB);
        }
    }
    TICK(ta[2]);
    {
        const lds_ptr d = c.lds + L_KA + gi * 4096 + u * KB + lane * 16;
#pragma unroll
        for (int i = 0; i < 5; ++i) *(LAS u32x4*)(d + i * 12288 + (i == 4 ? 128 : 0)) = v[i];
        *(LAS u32x4*)(c.lds + (u < 2 ? L_KT + gi * 2048 + u * KB : L_JUNK) + lane * 16) = v[5];
    }
    TICK(ta[3]);
    BAR_LGKM();
    STAMP(c, L, 7);
    {
        Loader l = l0; l.lw = c.wave & 3; asm volatile("" : "+s"(l.lw)); asm volatile("" : "+s"(l.src[1]));
        if (c.wave >= 4) fill_range<SC.fb[EV_E3], SC.fe[EV_E3], false>(c, l); else fill_range<SC.fb[EV_E3], SC.fe[EV_E3], true>(c, l);
    }
    if (L == 0) {
        fill_h<0>(c, 0, c.wave); fill_h<1>(c, 0, c.wave); fill_h<2>(c, 0, c.wave); fill_h<3>(c, 0, c.wave);
    }
    TICK(ta[4]);
    const f32x16 o = g >= 2 ? attn_body<false>(c, qm, qt, b_old, b_diag, 0) : attn_body<true>(c, qm, qt, b_old, b_diag, g == 1 ? 4 : 8);
    TICK(ta[5]);
    STAMP(c, L, 8);
    auto lr = __builtin_amdgcn_permlane32_swap(__float_as_uint(o[12]), __float_as_uint(o[12]), false, false);
    const float inv = __builtin_amdgcn_rcpf(__uint_as_float(lr[0]));
    const u32x4 om = {pk_bf16(o[0] * inv, o[1] * inv), pk_bf16(o[2] * inv, o[3] * inv), pk_bf16(o[4] * inv, o[5] * inv), pk_bf16(o[6] * inv, o[7] * inv)};
    const u32x2 ot = {pk_bf16(o[8] * inv, o[9] * inv), pk_bf16(o[10] * inv, o[11] * inv)};
    const unsigned ob = WS_O + L * XB + (g * 8 + 2 * w) * 20 * KB;
    const int cm = 5 * c.j + 2 * hh + h, ct = 5 * c.j + 4;
    const unsigned tb = (qi >> 4) * 20 * KB + (qi & 15) * 16;
    st128(c, clean4(om), tb + (cm >> 2) * KB + (cm & 3) * 256, ob);
    st64(c, clean2(ot), live_off(h == hh, tb + (ct >> 2) * KB + (ct & 3) * 256 + hh * 8), ob);
    TICK(ta[6]);
#pragma unroll
    for (int i = 0; i < 7; ++i) TICKSTORE(c, L, 20 + i, ta[i]);
    const int fi = w * 32 + c.j * 2 + hh;
    if (L == 0) { BAR_ALL(); signal(c, L, 1, fi); }
    else BAR_LGKM();
    STAMP(c, L, 9);
}

template <int PH> DI void proj_compute(const Ctx& c0, int L) {
    const Ctx c = fresh(c0);
    constexpr int NS = PH == 3 ? 20 : 54, CB = PH == 3 ? C3 : C5, NB = 16, N0 = 4, N1 = 10;
    const int w = c.wave, lane = c.lane, tok = lane & 15, q = lane >> 4, th = c.j >> 3, c8 = c.j & 7;
    const int tt = c.g * 8 + 4 * th + w;
    const unsigned act = (PH == 3 ? WS_O + L * XB : WS_H + L * HB) + tt * NS * KB;
    const int xi = PH == 3 ? 2 * L : 2 * L + 1;
    if (PH == 3) {
        if (L == 0) await(c, flag_off(L, 1, c.g) + (((4 * th + w) >> 1) * 32 + (lane & 31)) * 4, lane < 32);
        else canary(c, (5 * ((lane & 31) >> 1) + 4) * 256 + 15 * 16 + (lane & 1) * 8, act);
        asm volatile("s_waitcnt vmcnt(0)" ::: "memory");
    } else if (HID_DELAY) __builtin_amdgcn_s_sleep(HID_DELAY);
    STAMP(c, L, PH == 3 ? 10 : 20);
    const unsigned vo = lane * 16;
#define BLK(s) (PH == 3 ? (s) : hid_block(s))
#define LDA(s) ld128(c, vo, act + BLK(s) * KB)
    u32x4 bf[NB];
#pragma unroll
    for (int s = 0; s < N0; ++s) bf[s] = LDA(s);
    BAR_LGKM();
    STAMP(c, L, PH == 3 ? 11 : 21);
#pragma unroll
    for (int s = N0; s < N1; ++s) bf[s] = LDA(s);
    const int sp = (5 * c8 + 1) >> 1, gt = (c8 & 1) ? 5 * c8 : 5 * c8 + 4;
    const unsigned sgl = (gt >> 1) * KB + (2 * (gt & 1) + (q >> 1)) * 256 + tok * 16 + (q & 1) * 8;
    const unsigned rb = WS_X + xi * XB + tt * 20 * KB;
    u32x4 r0, r1; u32x2 r2;
    const Ring ring = make_ring((lds_cptr)c.lds + lane * 16);
    bf16x8 a[2][5];
#pragma unroll
    for (int t = 0; t < 5; ++t) a[0][t] = ring_frag(ring, (SC.pos[CB] + t) * KB);
    f32x4 acc[5];
    unsigned mx = chk(bf[0], chk(bf[1], chk(bf[2], chk(bf[3], 0u))));
    ballot_t late = late_lanes(mx);
    static_for<0, NS>([&](auto s_) __attribute__((always_inline)) {
        constexpr int s = decltype(s_)::value;
        if constexpr (s % 4 == 0) {
            if constexpr (s + 3 < NS) refetch1(c, late, bf[s % NB], bf[(s + 1) % NB], bf[(s + 2) % NB], bf[(s + 3) % NB], vo,
                                               act + BLK(s) * KB, act + BLK(s + 1) * KB, act + BLK(s + 2) * KB, act + BLK(s + 3) * KB);
            else refetch1(c, late, bf[s % NB], bf[(s + 1) % NB], vo, act + BLK(s) * KB, act + BLK(s + 1) * KB);
            mx = 0;
        }
        if (s % 4 == 3 && s + 1 < NS) BAR_LGKM();
        SBAR();
#pragma unroll
        for (int t = 0; t < 5; ++t) {
            MF16(s == 0, acc[t], a[s & 1][t], as_frag(bf[s % NB]));
            if (s + 1 < NS) a[(s + 1) & 1][PROJ_REV ? 4 - t : t] = ring_frag(ring, (SC.pos[CB + (s + 1) / 4] + ((s + 1) % 4) * 5 + (PROJ_REV ? 4 - t : t)) * KB);
            if (t == 4 && s + 4 < NS) mx = chk(bf[(s + 4) % NB], mx);
            SBAR();
        }
#pragma unroll
        for (int k = (s == 0 ? N1 : win_upto(s - 1, NB, NS, 2, N1)); k < win_upto(s, NB, NS, 2, N1); ++k) bf[k % NB] = LDA(k);
        if (s == 6) { r0 = ld128(c, vo, rb + sp * KB); r1 = ld128(c, vo, rb + (sp + 1) * KB); r2 = ld64(c, sgl, rb); }
        if (s % 4 == 3 && s + 1 < NS) late = late_lanes(mx);
        SBAR();
    });
    unsigned long long tp[6] = {};
    TICK(tp[0]);
    MFMA_DONE();
#pragma unroll
    for (int t = 0; t < 5; ++t) PIN4(acc[t]);
#undef LDA
#undef BLK
    STAMP(c, L, PH == 3 ? 12 : 22);
    TICK(tp[1]);
    BAR_LGKM();
    TICK(tp[2]);
    f32x4 y[5];
    {
        const unsigned rr[10] = {r0[0], r0[1], r0[2], r0[3], r1[0], r1[1], r1[2], r1[3], r2[0], r2[1]};
#pragma unroll
        for (int t = 0; t < 5; ++t) y[t] = acc[t] + (f32x4){bf_lo(rr[2 * t]), bf_hi(rr[2 * t]), bf_lo(rr[2 * t + 1]), bf_hi(rr[2 * t + 1])};
    }
    unsigned pk[10];
#pragma unroll
    for (int t = 0; t < 5; ++t) { pk[2 * t] = pk_bf16(y[t][0], y[t][1]); pk[2 * t + 1] = pk_bf16(y[t][2], y[t][3]); }
    if (PH == 5 && L == 1) {
        u16* orow_ = c.out + (size_t)(tt * 16 + tok) * DM;
        *(u32x4*)(orow_ + 32 * sp + 8 * q) = (u32x4){pk[0], pk[1], pk[2], pk[3]};
        *(u32x4*)(orow_ + 32 * (sp + 1) + 8 * q) = (u32x4){pk[4], pk[5], pk[6], pk[7]};
        *(u32x2*)(orow_ + 16 * gt + 4 * q) = (u32x2){pk[8], pk[9]};
        BAR_LGKM();
        STAMP(c, L, 24);
    } else {
        const unsigned ob = WS_X + (xi + 1) * XB + tt * 20 * KB;
#pragma unroll
        for (int i = 0; i < 10; ++i) pk[i] = clean(pk[i]);
        st128(c, (u32x4){pk[0], pk[1], pk[2], pk[3]}, vo, ob + sp * KB);
        st128(c, (u32x4){pk[4], pk[5], pk[6], pk[7]}, vo, ob + (sp + 1) * KB);
        TICK(tp[3]);
        st64(c, (u32x2){pk[8], pk[9]}, sgl, ob);
        TICK(tp[4]);
        const f32x4 s4 = (y[0] + y[1]) + (y[2] + y[3]) + y[4];
        const float mean = sum_q((s4[0] + s4[1]) + (s4[2] + s4[3])) * (1.f / 80.f);
        const f32x2 nm = splat2(-mean);
        f32x2 q0 = {0.f, 0.f}, q1 = {0.f, 0.f};
#pragma unroll
        for (int t = 0; t < 5; ++t) { const f32x2 d0 = lo2(y[t]) + nm, d1 = hi2(y[t]) + nm; q0 = fma2(d0, d0, q0); q1 = fma2(d1, d1, q1); }
        const float m2 = sum_q((q0[0] + q0[1]) + (q1[0] + q1[1]));
        st64(c, (u32x2){clean(__float_as_uint(mean)), clean(__float_as_uint(m2))},
             live_off(q == 0, ((c.g * 8 + c8) * 128 + 64 * th + 16 * w + tok) * 8), WS_ST + (xi + 1) * STB);
        TICK(tp[5]);
        if (PH == 3) {
#pragma unroll
            for (int i = 0; i < 6; ++i) TICKSTORE(c, L, 14 + i, tp[i]);
        }
        STAMP(c, L, PH == 3 ? 13 : 23);
        BAR_LGKM();
        STAMP(c, L, PH == 3 ? 14 : 24);
    }
}

DI void p4_compute(const Ctx& c0, int L) {
    const Ctx c = fresh(c0);
    constexpr int NB = NB4;
    const int w = c.wave, lane = c.lane, tok = lane & 15, q = lane >> 4;
    const unsigned xin = WS_X + (2 * L + 1) * XB + (c.g * 8 + 2 * w) * 20 * KB, stin = WS_ST + (2 * L + 1) * STB + c.g * 8 * 128 * 8;
    Stats st;
    stats_wait(c, stin, true, st);
    STAMP(c, L, 15);
#define LDX(s, m) ld128(c, lane * 16 + (m) * 20 * KB, xin + (s) * KB)
    u32x4 bf[NB][2];
#pragma unroll
    for (int s = 0; s < 2; ++s)
#pragma unroll
        for (int m = 0; m < 2; ++m) bf[s][m] = LDX(s, m);
    BAR_LGKM();
    STAMP(c, L, 16);
#pragma unroll
    for (int s = 2; s < WIN0; ++s)
#pragma unroll
        for (int m = 0; m < 2; ++m) bf[s][m] = LDX(s, m);
    unsigned long long tk[8] = {};
    TICK(tk[0]);
    const Ring ring = make_ring((lds_cptr)c.lds + lane * 16);
    bf16x8 a[2][7];
#pragma unroll
    for (int t = 0; t < 7; ++t) a[0][t] = ring_frag(ring, (SC.pos[C4] + t) * KB);
    float mean[2], var[2], gl[2];
    stats_reduce(st, mean, var);
#pragma unroll
    for (int m = 0; m < 2; ++m) { gl[m] = -1.4426950408889634f * __builtin_amdgcn_rsqf(var[m]); asm volatile("" : "+v"(mean[m]), "+v"(var[m]), "+v"(gl[m])); }
    f32x4 csg[7], csu[7];
    f32x4 acc[2][7][2];
    constexpr int G = 2;
    unsigned mx = 0;
#pragma unroll
    for (int i = 0; i < G; ++i) mx = chk(bf[i][0], chk(bf[i][1], mx));
    ballot_t late = late_lanes(mx);
    static_for<0, 40>([&](auto hs_) __attribute__((always_inline)) {
        constexpr int hs = decltype(hs_)::value, s = hs >> 1, part = hs & 1;
        if (!part && s % G == 0) {
            refetch(c, late, bf[s % NB], bf[(s + 1) % NB], lane * 16, lane * 16 + 20 * KB, xin + s * KB);
            mx = 0;
        }
        if (part && hs + 1 < 40) BAR_LGKM();
        if (hs == 1) TICK(tk[1]); if (hs == 3) TICK(tk[2]); if (hs == 11) TICK(tk[3]); if (hs == 21) TICK(tk[4]); if (hs == 31) TICK(tk[5]); if (hs == 37) TICK(tk[6]);
        SBAR();
#pragma unroll
        for (int t = 0; t < 7; ++t) {
            MF16(s == 0, acc[part][t][0], a[hs & 1][t], as_frag(bf[s % NB][0]));
            if (hs + 1 < 40 && t < 4) a[(hs + 1) & 1][6 - 2 * t] = ring_frag(ring, (SC.pos[C4 + (hs + 1) / 2] + ((hs + 1) & 1) * 7 + 6 - 2 * t) * KB);
            if (hs == 39) {
                csg[t] = *(const LAS f32x4*)((lds_cptr)c.lds + L_CS13 + (t * 16 + 4 * q) * 4);
                csu[t] = *(const LAS f32x4*)((lds_cptr)c.lds + L_CS13 + ((7 + t) * 16 + 4 * q) * 4);
            }
            SBAR();
            MF16(s == 0, acc[part][t][1], a[hs & 1][t], as_frag(bf[s % NB][1]));
            if (hs + 1 < 40 && t < 3) a[(hs + 1) & 1][5 - 2 * t] = ring_frag(ring, (SC.pos[C4 + (hs + 1) / 2] + ((hs + 1) & 1) * 7 + 5 - 2 * t) * KB);
            if (part && (s / G + 1) * G + s % G < 20 && t >= 5) mx = chk(bf[((s / G + 1) * G + s % G) % NB][t & 1], mx);
            SBAR();
        }
        if (part) {
#pragma unroll
            for (int k = (s == 0 ? WIN0 : win_upto(s - 1, NB, 20, 2)); k < win_upto(s, NB, 20, 2); ++k)
#pragma unroll
                for (int m = 0; m < 2; ++m) bf[k % NB][m] = LDX(k, m);
        }
        if (part && s % G == G - 1 && s + 1 < 20) late = late_lanes(mx);
        SBAR();
    });
    MFMA_DONE();
#pragma unroll
    for (int t = 0; t < 14; ++t) { PIN4(acc[t / 7][t % 7][0]); PIN4(acc[t / 7][t % 7][1]); }

#undef LDX
    TICK(tk[7]);
#pragma unroll
    for (int i = 0; i < 8; ++i) TICKSTORE(c, L, i, tk[i]);
    STAMP(c, L, 17);
    const int sp = (7 * c.j + 1) >> 1, gt = (c.j & 1) ? 7 * c.j : 7 * c.j + 6;
    const unsigned sgl = (gt >> 1) * KB + (2 * (gt & 1) + (q >> 1)) * 256 + tok * 16 + (q & 1) * 8;
    const unsigned hb = WS_H + L * HB + (c.g * 8 + 2 * w) * 54 * KB;
    u32x2 hp[2][2];
    const f32x2 nm2[2] = {splat2(-mean[0]), splat2(-mean[1])}, gl2[2] = {splat2(gl[0]), splat2(gl[1])}, var2[2] = {splat2(var[0]), splat2(var[1])};
#pragma unroll
    for (int t = 0; t < 7; ++t)
#pragma unroll
        for (int m = 0; m < 2; ++m) {
#pragma unroll
            for (int hh = 0; hh < 2; ++hh) {
                const f32x2 ag = fma2(hh ? hi2(csg[t]) : lo2(csg[t]), nm2[m], hh ? hi2(acc[0][t][m]) : lo2(acc[0][t][m]));
                const f32x2 au = fma2(hh ? hi2(csu[t]) : lo2(csu[t]), nm2[m], hh ? hi2(acc[1][t][m]) : lo2(acc[1][t][m]));
                const f32x2 x = ag * gl2[m];
                const f32x2 d = fma2((f32x2){__builtin_amdgcn_exp2f(x[0]), __builtin_amdgcn_exp2f(x[1])}, var2[m], var2[m]);
                const f32x2 hv = (ag * au) * (f32x2){__builtin_amdgcn_rcpf(d[0]), __builtin_amdgcn_rcpf(d[1])};
                hp[t & 1][m][hh] = clean(pk_bf16(hv[0], hv[1]));
            }
            if (t < 6 && (t & 1)) st128(c, (u32x4){hp[0][m][0], hp[0][m][1], hp[1][m][0], hp[1][m][1]}, live_off(sp + (t >> 1) < 54, lane * 16), hb + (m * 54 + min(sp + (t >> 1), 53)) * KB);
            if (t == 6) st64(c, hp[0][m], sgl, hb + m * 54 * KB);
        }
    STAMP(c, L, 18);
    BAR_LGKM();
    STAMP(c, L, 19);
}

__global__ __launch_bounds__(512, 2) void main_kernel(MainArgs a) {
    extern __shared__ __attribute__((aligned(16))) char smem[];
    Ctx c;
    c.ws = a.ws; c.out = a.out;
    c.rs = __builtin_amdgcn_make_buffer_rsrc(a.ws, 0, (int)WS_END, 0x00020000);
    c.lds = (lds_ptr)smem; c.lds0 = (unsigned)(uintptr_t)c.lds;
    { const int b = blockIdx.x, xcd = b & 7, idx = b >> 3; c.g = xcd * 2 + (idx >> 4); c.j = idx & 15; }
    c.wave = __builtin_amdgcn_readfirstlane(threadIdx.x >> 6); c.lane = threadIdx.x & 63;
    Loader l; l.lw = c.wave - 4; l.voff = c.lane * 16;
    l.src[0] = WS_WQ + c.j * 160 * KB; l.src[1] = WS_WO + (c.j & 7) * 100 * KB; l.src[2] = WS_W13 + c.j * 280 * KB; l.src[3] = WS_W2 + (c.j & 7) * 270 * KB;
    if (c.wave >= 4) fill_range<0, PRO>(c, l, false);
    if (PRIO_C && c.wave < 4) __builtin_amdgcn_s_setprio(PRIO_C);
    for (int L = 0; L < 2; ++L) {
        if (c.wave < 4) p1_compute(c, L); else loader_events<EV_B1, EV_E1 + 1>(c, l, L);
        if (PRIO_A == 1) __builtin_amdgcn_s_setprio(0);
        if (PRIO_A == 2) { if (c.wave >= 4) __builtin_amdgcn_s_setprio(1); else __builtin_amdgcn_s_setprio(0); }
        p2_attention(c, l, L);
        if (PRIO_A) { if (c.wave < 4) __builtin_amdgcn_s_setprio(PRIO_C); else __builtin_amdgcn_s_setprio(0); }
        if (c.wave < 4) { proj_compute<3>(c, L); p4_compute(c, L); proj_compute<5>(c, L); }
        else loader_events<EV_B3, NEV>(c, l, L);
    }
    asm volatile("s_waitcnt vmcnt(0)" ::: "memory");
    asm volatile(".subsection 2\n\t.fill 128, 4, 0xBF800000\n\t.subsection 0");
}

extern "C" void kernel_launch(void* const* d_in, const int* in_sizes, int n_in, void* d_out, int out_size, void* d_ws, size_t ws_size, hipStream_t stream) {
    PrepArgs p;
    p.x = (const u16*)d_in[0]; p.wqkv = (const u16*)d_in[1]; p.wo = (const u16*)d_in[2];
    p.w1 = (const u16*)d_in[4]; p.w3 = (const u16*)d_in[5]; p.w2 = (const u16*)d_in[6]; p.ws = (char*)d_ws;
    prep_kernel<<<PREP_UNITS, 256, 0, stream>>>(p);
    MainArgs m; m.ws = (char*)d_ws; m.out = (u16*)d_out;
    (void)hipFuncSetAttribute((const void*)main_kernel, hipFuncAttributeMaxDynamicSharedMemorySize, (int)LDS_BYTES);
    main_kernel<<<256, 512, LDS_BYTES, stream>>>(m);
}
```
